```python
import torch

def _softmax(x, axis=-1):
    return torch.softmax(x, dim=axis)
def _gelu(x):
    return torch.nn.functional.gelu(x, approximate="tanh")
def _silu(x):
    return torch.nn.functional.silu(x)
def _sigmoid(x):
    return torch.sigmoid(x)
def _layernorm(x, axis=-1, eps=1e-5):
    m = x.mean(dim=axis, keepdim=True)
    v = x.var(dim=axis, keepdim=True, unbiased=False)
    return (x - m) / torch.sqrt(v + eps)
def _scatter_add(tgt, idx, src, axis=0):
    return tgt.clone().index_add_(axis, idx, src)
def _segment_sum(src, seg, n):
    out = torch.zeros((n,) + src.shape[1:], dtype=src.dtype)
    return out.index_add_(0, seg, src)
def _pad(x, pw):
    flat = tuple(v for pair in reversed(pw) for v in pair)
    return torch.nn.functional.pad(x, flat)

kernel_name = 'interleave_layers__sliding_block_cached__dense_activation_silu__decode'

def setup_inputs():

    _w = (lambda a: (lambda t: t.to(torch.bfloat16) if t.is_floating_point() else t)(torch.as_tensor(a)))
    def _A_setup():
        B, Sq, D = 4, 1, 512
        Hq, Hkv, Sk, Dh = 32, 8, 4096, 128
        assert Hq % Hkv == 0, (Hq, Hkv)
        return {
            "x": _w((lambda *s: torch.randn(*s, dtype=torch.bfloat16) * 0.1)(B, Sq, D)),
            "k_cache": _w((lambda *s: torch.randn(*s, dtype=torch.bfloat16) * 0.8)(B, Hkv, Sk, Dh)),
            "v_cache": _w((lambda *s: torch.randn(*s, dtype=torch.bfloat16) * 3.2)(B, Hkv, Sk, Dh)),
            "wq": _w((lambda *s: torch.randn(*s, dtype=torch.bfloat16) * 0.8)(D, Hq * Dh) * (D ** -0.5)),
            "wo": _w((lambda *s: torch.randn(*s, dtype=torch.bfloat16) * 3.2)(Hq * Dh, D) * ((Hq * Dh) ** -0.5)),
        }
    _A_in = _A_setup()

    def _B_setup():
        B, S, D, F = 4, 1, 512, 1792
        return {
            "x": _w((lambda *s: torch.randn(*s, dtype=torch.bfloat16) * 0.1)(B, S, D)),
            "w1": _w((lambda *s: torch.randn(*s, dtype=torch.bfloat16) * 1.6)(D, F) * (D ** -0.5)),
            "w2": _w((lambda *s: torch.randn(*s, dtype=torch.bfloat16) * 0.4)(F, D) * (F ** -0.5)),
        }
    _B_in = _B_setup()

    return {
        "A_x": _A_in["x"],
        "A_k_cache": _A_in["k_cache"],
        "A_v_cache": _A_in["v_cache"],
        "A_wq": _A_in["wq"],
        "A_wo": _A_in["wo"],
        "B_x": _B_in["x"],
        "B_w1": _B_in["w1"],
        "B_w2": _B_in["w2"],
    }

def reference(A_x, A_k_cache, A_v_cache, A_wq, A_wo, B_x, B_w1, B_w2):
    def _A_ref(x, k_cache, v_cache, wq, wo):
        B, Sq, D = x.shape
        Hq, Dh = 32, 128
        Hkv, Sk = k_cache.shape[1], k_cache.shape[2]
        W = min(4096, max(256, Sk // 4))
        rep = Hq // Hkv
        q = (lambda x, *d: x.permute(*d))((x @ wq).reshape(B, Sq, Hq, Dh), 0, 2, 1, 3)
        k = (lambda x, n, axis: torch.repeat_interleave(x, n, dim=axis))(k_cache[:, :, Sk - W:, :], rep, axis=1)
        v = (lambda x, n, axis: torch.repeat_interleave(x, n, dim=axis))(v_cache[:, :, Sk - W:, :], rep, axis=1)
        scores = torch.einsum("bhqd,bhkd->bhqk", q, k) * (Dh ** -0.5)
        p = _softmax(scores, axis=-1)
        o = torch.einsum("bhqk,bhkd->bhqd", p, v)
        return (lambda x, *d: x.permute(*d))(o, 0, 2, 1, 3).reshape(B, Sq, Hq * Dh) @ wo

    def _B_ref(x, w1, w2):
        h = torch.einsum("bsd,df->bsf", x, w1)
        h = _silu(h)
        out = torch.einsum("bsf,fd->bsd", h, w2)
        return out

    x = A_x
    x = x + _A_ref(_layernorm(x, axis=-1), A_k_cache, A_v_cache, A_wq, A_wo)
    x = x + _B_ref(_layernorm(x, axis=-1), B_w1, B_w2)
    x = x + _A_ref(_layernorm(x, axis=-1), A_k_cache, A_v_cache, A_wq, A_wo)
    x = x + _B_ref(_layernorm(x, axis=-1), B_w1, B_w2)
    return x
```

```cpp
#include <hip/hip_runtime.h>
#include <stdint.h>

#define LAS __attribute__((address_space(3)))
#define GAS __attribute__((address_space(1)))
typedef unsigned v4u __attribute__((ext_vector_type(4)));
typedef unsigned v2u __attribute__((ext_vector_type(2)));
typedef float f32x4 __attribute__((ext_vector_type(4)));
typedef float f32x2 __attribute__((ext_vector_type(2)));
typedef short bf16x8 __attribute__((ext_vector_type(8)));
typedef short s16x4 __attribute__((ext_vector_type(4)));
typedef __bf16 bf16x2 __attribute__((ext_vector_type(2)));
typedef unsigned long long u64;

namespace {

constexpr int NWG = 256, NTHR = 576;
constexpr unsigned SENT = 0xFFFFFFFFu;
constexpr int SPIN_CAP = 1 << 20;
constexpr int AUX_SC1 = 16;

constexpr int WS_EPOCH = 0;
constexpr int WS_FL = 1024;
constexpr int WS_QO = 4096, QO_STRIDE = 2048;
constexpr int WS_H = WS_QO + 64 * QO_STRIDE, H_STRIDE = 1024;
constexpr int WS_X2 = WS_H + 32 * H_STRIDE;
constexpr int WS_PO = WS_X2 + 32 * 128, PO_STRIDE = 2304, PO_LAYER = 1152;
constexpr int CLR_CHUNK = 7296;
constexpr int WS_END = WS_QO + 64 * CLR_CHUNK;
static_assert(WS_PO + 128 * PO_STRIDE <= WS_END, "preset range");

constexpr int L_WA = 0, L_WB = 65536;
constexpr int L_K = 0, L_V = 65536;
constexpr int L_QL = 131072, L_PL = L_QL + 1280, L_ST = L_PL + 2048;
constexpr int L_RED = 131072, RED_BUF = 10496, RED_CS = 8192, RED_G = 10240;
constexpr int L_ACT = L_RED + 2 * RED_BUF, ACT_ROW = 1088;
constexpr int L_CTL = L_ACT + 4 * ACT_ROW;
constexpr int L_SCR = L_CTL + 64;
constexpr int LDS_BYTES = L_SCR + 1024;
static_assert(LDS_BYTES <= 160 * 1024, "LDS map");

struct Params {
  const unsigned short* x;
  const unsigned char* kc;
  const unsigned char* vc;
  const unsigned char* wq;
  const unsigned char* wo;
  const unsigned char* w1;
  const unsigned char* w2;
  unsigned* out;
  unsigned char* ws;
};

__device__ __forceinline__ f32x4 mfma(v4u a, v4u b, f32x4 c) {
  return __builtin_amdgcn_mfma_f32_16x16x32_bf16(__builtin_bit_cast(bf16x8, a), __builtin_bit_cast(bf16x8, b), c, 0, 0, 0);
}
__device__ __forceinline__ unsigned pk(float a, float b) {
  bf16x2 v = {(__bf16)a, (__bf16)b};
  return __builtin_bit_cast(unsigned, v);
}
__device__ __forceinline__ unsigned canon(unsigned u) { return u == SENT ? 0x7FC07FC0u : u; }
__device__ __forceinline__ float bflo(unsigned u) { return __uint_as_float(u << 16); }
__device__ __forceinline__ float bfhi(unsigned u) { return __uint_as_float(u & 0xffff0000u); }
__device__ __forceinline__ unsigned mx4(v4u x) { return max(max(x.x, x.y), max(x.z, x.w)); }
__device__ __forceinline__ bool all_lanes(bool ok) { return __builtin_amdgcn_ballot_w64(!ok) == 0ull; }
template <int CTRL> __device__ __forceinline__ unsigned dppu(unsigned x) { return (unsigned)__builtin_amdgcn_mov_dpp((int)x, CTRL, 0xf, 0xf, true); }
template <int CTRL> __device__ __forceinline__ float dpp(float x) { return __uint_as_float(dppu<CTRL>(__float_as_uint(x))); }
constexpr int ROR8 = 0x128, XOR1 = 0xB1, XOR2 = 0x4E, HMIR = 0x141;
__device__ __forceinline__ float fmx(float a, float b) { return __builtin_elementwise_maximum(a, b); }
__device__ __forceinline__ v4u quad_frag(unsigned d) { return v4u{dppu<0x00>(d), dppu<0x55>(d), dppu<0xAA>(d), dppu<0xFF>(d)}; }
__device__ __forceinline__ float xrow_max(float x) {
  auto s = __builtin_amdgcn_permlane16_swap(__float_as_uint(x), __float_as_uint(x), false, false);
  x = fmx(__uint_as_float(s[0]), __uint_as_float(s[1]));
  auto t = __builtin_amdgcn_permlane32_swap(__float_as_uint(x), __float_as_uint(x), false, false);
  return fmx(__uint_as_float(t[0]), __uint_as_float(t[1]));
}
__device__ __forceinline__ float xrow_sum(float x) {
  auto s = __builtin_amdgcn_permlane16_swap(__float_as_uint(x), __float_as_uint(x), false, false);
  x = __uint_as_float(s[0]) + __uint_as_float(s[1]);
  auto t = __builtin_amdgcn_permlane32_swap(__float_as_uint(x), __float_as_uint(x), false, false);
  return __uint_as_float(t[0]) + __uint_as_float(t[1]);
}
__device__ __forceinline__ float swap32_add(float a, float b) {
  auto t = __builtin_amdgcn_permlane32_swap(__float_as_uint(a), __float_as_uint(b), false, false);
  return __uint_as_float(t[0]) + __uint_as_float(t[1]);
}
__device__ __forceinline__ float swap16_add(float a, float b) {
  auto t = __builtin_amdgcn_permlane16_swap(__float_as_uint(a), __float_as_uint(b), false, false);
  return __uint_as_float(t[0]) + __uint_as_float(t[1]);
}
__device__ __forceinline__ void dma16(const void* gbase, unsigned voff, unsigned dst) {
  unsigned keep;
  asm volatile("s_mov_b32 %0, m0\n\ts_mov_b32 m0, %3\n\ts_nop 0\n\tglobal_load_lds_dwordx4 %1, %2 nt\n\ts_mov_b32 m0, %0"
               : "=&s"(keep) : "v"(voff), "s"(gbase), "s"(dst) : "memory");
}
__device__ __forceinline__ v2u trread(LAS unsigned char* p) {
  return __builtin_bit_cast(v2u, __builtin_amdgcn_ds_read_tr16_b64_v4i16((LAS s16x4*)p));
}
__device__ __forceinline__ v4u trfrag(LAS unsigned char* p0, LAS unsigned char* p1) {
  const v2u a = trread(p0), b = trread(p1);
  return v4u{a.x, a.y, b.x, b.y};
}
#define WAIT_VM(n) asm volatile("s_waitcnt vmcnt(" #n ")" ::: "memory")
#define WAIT_LGKM0() asm volatile("s_waitcnt lgkmcnt(0)" ::: "memory")
#define BAR() do { WAIT_LGKM0(); __builtin_amdgcn_s_barrier(); asm volatile("" ::: "memory"); } while (0)
#define FRESH() asm volatile("" ::: "memory")

__device__ __forceinline__ v4u ld16(__amdgpu_buffer_rsrc_t rs, int off) { return __builtin_amdgcn_raw_buffer_load_b128(rs, off, 0, AUX_SC1); }
__device__ __forceinline__ v2u ld8(__amdgpu_buffer_rsrc_t rs, int off) { return __builtin_amdgcn_raw_buffer_load_b64(rs, off, 0, AUX_SC1); }
__device__ __forceinline__ unsigned ld4(__amdgpu_buffer_rsrc_t rs, int off) { return __builtin_amdgcn_raw_buffer_load_b32(rs, off, 0, AUX_SC1); }
__device__ __forceinline__ void st16(__amdgpu_buffer_rsrc_t rs, int off, v4u v) { __builtin_amdgcn_raw_buffer_store_b128(v, rs, off, 0, AUX_SC1); }
__device__ __forceinline__ void st8(__amdgpu_buffer_rsrc_t rs, int off, v2u v) { __builtin_amdgcn_raw_buffer_store_b64(v, rs, off, 0, AUX_SC1); }
__device__ __forceinline__ void st4(__amdgpu_buffer_rsrc_t rs, int off, unsigned v) { __builtin_amdgcn_raw_buffer_store_b32(v, rs, off, 0, AUX_SC1); }

__device__ __forceinline__ void ctl_wait(volatile LAS unsigned* ctl, unsigned v) {
  _Pragma("nounroll") for (int i = 0; i < SPIN_CAP * 4; ++i) { if (*ctl >= v) break; __builtin_amdgcn_s_sleep(1); }
  asm volatile("" ::: "memory");
}
template <int N> __device__ __forceinline__ void pace() {
#pragma unroll
  for (int i = 0; i < N / 120; ++i) __builtin_amdgcn_s_sleep(120);
  if (N % 120) __builtin_amdgcn_s_sleep(N % 120);
}
#define BACKOFF() __builtin_amdgcn_s_sleep(POLL_SLEEP)

#define STAMP(i) do {} while (0)
#define NPASS(i, n) do {} while (0)
#define FSTAMP(i) do {} while (0)
#define STAMP_DUMP() do {} while (0)

#define LAYER_UNROLL 2
#define POLL_SLEEP 2
#define DLY_P 36
#define DLY_A1 200
#define DLY_O1 70
#define DLY_E 90
#define DLY_C0 0
#define DLY_C1 150
#define DLY_X1 40
#define DLY_D0 35
#define DLY_FC1 150
#define DLY_D1 25
#define DLY_WO 10
#define DLY_W1 120
#define DLY_W2 85

__device__ __forceinline__ v2u read_tag(__amdgpu_buffer_rsrc_t rs) {
  const v2u e = ld8(rs, WS_EPOCH);
  const u64 tag = (((u64)e.y << 32) | e.x) + 1ull;
  return v2u{(unsigned)tag, (unsigned)(tag >> 32)};
}
__device__ __forceinline__ void wait_preset(__amdgpu_buffer_rsrc_t rs, int lane, v2u tg) {
  pace<DLY_P>();
  _Pragma("nounroll") for (int it = 0; it < SPIN_CAP; ++it) {
    FRESH();
    const v4u a = ld16(rs, WS_FL + (lane & 31) * 16);
    if (all_lanes(a.x == tg.x && a.y == tg.y && a.z == tg.x && a.w == tg.y)) break;
    BACKOFF();
  }
}
__device__ __forceinline__ void preset_chunk(__amdgpu_buffer_rsrc_t rs, int chunk, int tid) {
  if (tid < CLR_CHUNK / 16) st16(rs, WS_QO + chunk * CLR_CHUNK + tid * 16, v4u{SENT, SENT, SENT, SENT});
  WAIT_VM(0);
  BAR();
}
__device__ __forceinline__ void control_announce(LAS unsigned char* L, __amdgpu_buffer_rsrc_t rs, int lane, int chunk) {
  volatile LAS unsigned* ctl = (volatile LAS unsigned*)(L + L_CTL);
  const v2u tg = read_tag(rs);
  BAR();
  if (lane == 0) st8(rs, WS_FL + chunk * 8, tg);
  wait_preset(rs, lane, tg);
  if (lane == 0) ctl[0] = 2u;
}

__device__ __forceinline__ void wslice_dma(const unsigned char* W, unsigned rowbytes, unsigned row0, unsigned colbyte, unsigned dst, int lane) {
#pragma unroll
  for (int i = 0; i < 8; ++i) {
    const int r = 8 * i + (lane >> 3);
    const int sw = (((r >> 1) & 1) | (((r >> 3) & 1) << 1)) << 1;
    dma16(W, (row0 + r) * rowbytes + colbyte + (((lane & 7) ^ sw) << 4), dst + i * 1024);
  }
}
__device__ __forceinline__ void wslice_frags(LAS unsigned char* base, int lane, v4u (&f)[2][4]) {
  const int grp = lane >> 4, q = (lane & 15) >> 2, pp = lane & 3;
#pragma unroll
  for (int ks = 0; ks < 2; ++ks)
#pragma unroll
    for (int nt = 0; nt < 4; ++nt) {
      v2u h[2];
#pragma unroll
      for (int t = 0; t < 2; ++t) {
        const int r = 32 * ks + 8 * grp + 4 * t + q;
        const int sw = (((r >> 1) & 1) | (((r >> 3) & 1) << 1)) << 1;
        h[t] = trread(base + r * 128 + (((2 * nt + (pp >> 1)) ^ sw) << 4) + (pp & 1) * 8);
      }
      f[ks][nt] = v4u{h[0].x, h[0].y, h[1].x, h[1].y};
    }
}

__device__ __forceinline__ void ln_proj_partial(LAS unsigned char* R, int w, int lane, const v4u (&xa)[2], const v4u (&wf)[2][4]) {
  const int c16 = lane & 15, grp = lane >> 4;
  const v4u ones = {0x3F803F80u, 0x3F803F80u, 0x3F803F80u, 0x3F803F80u};
  const f32x4 z = {0.f, 0.f, 0.f, 0.f};
  f32x4 acc[4];
#pragma unroll
  for (int nt = 0; nt < 4; ++nt) acc[nt] = mfma(xa[1], wf[1][nt], mfma(xa[0], wf[0][nt], z));
  const f32x4 gr = mfma(xa[1], xa[1], mfma(xa[0], xa[0], z));
  const f32x4 sx = mfma(xa[1], ones, mfma(xa[0], ones, z));
#pragma unroll
  for (int nt = 0; nt < 4; ++nt) *(LAS float*)(R + w * 1024 + grp * 256 + (16 * nt + c16) * 4) = acc[nt][0];
  if (c16 == 4 * grp) *(LAS f32x2*)(R + RED_G + w * 32 + grp * 8) = f32x2{gr[0], sx[0]};
}
__device__ __forceinline__ void colsum_partial(LAS unsigned char* R, int w, int lane, const v4u (&wf)[2][4]) {
  const v4u ones = {0x3F803F80u, 0x3F803F80u, 0x3F803F80u, 0x3F803F80u};
  const f32x4 z = {0.f, 0.f, 0.f, 0.f};
#pragma unroll
  for (int nt = 0; nt < 4; ++nt) {
    const f32x4 c = mfma(ones, wf[1][nt], mfma(ones, wf[0][nt], z));
    if (lane < 16) *(LAS float*)(R + RED_CS + w * 256 + (16 * nt + lane) * 4) = c[0];
  }
}
__device__ __forceinline__ float colsum_final(LAS unsigned char* R, int lane) {
  float c = 0.f;
#pragma unroll
  for (int v = 0; v < 8; ++v) c += *(LAS float*)(R + RED_CS + v * 256 + lane * 4);
  return c;
}
__device__ __forceinline__ float ln_proj_final(LAS unsigned char* R, int b, int lane, float cs) {
  float t[8];
#pragma unroll
  for (int v = 0; v < 8; ++v) t[v] = *(LAS float*)(R + v * 1024 + b * 256 + lane * 4);
  f32x2 g = *(LAS f32x2*)(R + RED_G + (lane & 7) * 32 + b * 8);
  __builtin_amdgcn_sched_barrier(0);
  g.x += dpp<XOR1>(g.x); g.y += dpp<XOR1>(g.y);
  g.x += dpp<XOR2>(g.x); g.y += dpp<XOR2>(g.y);
  g.x += dpp<HMIR>(g.x); g.y += dpp<HMIR>(g.y);
  const float mu = g.y * (1.0f / 512.0f);
  const float var = fmaxf(g.x * (1.0f / 512.0f) - mu * mu, 0.0f);
  const float rstd = __builtin_amdgcn_rsqf(var + 1e-5f);
  const float y = ((t[0] + t[1]) + (t[2] + t[3])) + ((t[4] + t[5]) + (t[6] + t[7]));
  return rstd * (y - mu * cs);
}
__device__ __forceinline__ void publish_pairs(__amdgpu_buffer_rsrc_t rs, int off, int lane, float v) {
  const unsigned o = canon(pk(v, dpp<XOR1>(v)));
  if ((lane & 1) == 0) st4(rs, off + lane * 2, o);
}

__device__ __forceinline__ void qo_role(const Params& p, LAS unsigned char* L, unsigned lbase, __amdgpu_buffer_rsrc_t rs, int g, int j, int w, int lane) {
  volatile LAS unsigned* ctl = (volatile LAS unsigned*)(L + L_CTL);
  const int c16 = lane & 15, grp = lane >> 4;
  const int qid = g * 8 + j;
  const int myout = WS_QO + qid * QO_STRIDE;

  if (w == 8) {
    const v2u tg = read_tag(rs);
    BAR();
    if (lane == 0) { ctl[2] = tg.x; ctl[3] = tg.y; }
    wait_preset(rs, lane, tg);
    if (lane == 0) ctl[0] = 2u;
    STAMP(2);
    STAMP_DUMP();
    return;
  }
  BAR();
  STAMP(0);
  wslice_dma(p.wq, 8192, 64 * w, (g * 512 + 64 * j) * 2, lbase + L_WA + w * 8192, lane);
  v4u xa[2];
#pragma unroll
  for (int ks = 0; ks < 2; ++ks) xa[ks] = *(const GAS v4u*)(p.x + (c16 >> 2) * 512 + 64 * w + 32 * ks + 8 * grp);
  WAIT_VM(0);
  v4u wq[2][4], wo[2][4];
  wslice_frags(L + L_WA + w * 8192, lane, wq);
  float csq = 0.f;
  int rb = 0;

#pragma unroll LAYER_UNROLL
  for (int layer = 0; layer < 2; ++layer) {
    {
      LAS unsigned char* R = L + L_RED + rb; rb ^= RED_BUF;
      if (layer == 1) {
        pace<DLY_E>();
        unsigned d[2];
        int it = 0;
        _Pragma("nounroll") for (; it < SPIN_CAP; ++it) {
          FRESH();
#pragma unroll
          for (int ks = 0; ks < 2; ++ks)
            d[ks] = ld4(rs, WS_X2 + (4 * w + 2 * ks + (grp >> 1)) * 128 + (c16 >> 2) * 32 + (grp & 1) * 16 + (c16 & 3) * 4);
          if (all_lanes(max(d[0], d[1]) != SENT)) break;
        }
        STAMP(10); NPASS(20, it);
        xa[0] = quad_frag(d[0]); xa[1] = quad_frag(d[1]);
      }
      ln_proj_partial(R, w, lane, xa, wq);
      if (layer == 0) colsum_partial(R, w, lane, wq);
      BAR();
      if (w < 4) {
        if (layer == 0) csq = colsum_final(R, lane);
        const float y = ln_proj_final(R, w, lane, csq);
        if (layer == 0) ctl_wait(ctl, 2u);
        publish_pairs(rs, myout + layer * 512 + w * 128, lane, y);
        STAMP(layer == 0 ? 1 : 11);
      }
    }
    if (layer == 0) {
      pace<DLY_WO>();
      wslice_dma(p.wo, 1024, 512 * g + 64 * w, 128 * j, lbase + L_WB + w * 8192, lane);
      WAIT_VM(0);
      wslice_frags(L + L_WB + w * 8192, lane, wo);
      STAMP(2);
      ctl_wait(ctl, 2u);
      STAMP(3);
    } else {
      pace<DLY_O1>();
    }
    {
      LAS unsigned char* R = L + L_RED + rb; rb ^= RED_BUF;
      const int hh = w >> 1;
      v4u oa[2];
      unsigned lw;
      const int po = WS_PO + (g * 16 + (c16 & 3) * 4) * PO_STRIDE + layer * PO_LAYER;
      if (layer == 1) STAMP(18);
      int it = 0;
      _Pragma("nounroll") for (; it < SPIN_CAP; ++it) {
        FRESH();
#pragma unroll
        for (int ks = 0; ks < 2; ++ks)
          oa[ks] = ld16(rs, po + (c16 >> 2) * PO_STRIDE + (4 * (w & 1) + 2 * ks + (grp >> 1)) * 128 + hh * 32 + (grp & 1) * 16);
        lw = ld4(rs, po + grp * PO_STRIDE + 1024 + hh * 4);
        if (all_lanes(max(max(mx4(oa[0]), mx4(oa[1])), lw) != SENT)) break;
        BACKOFF();
      }
      STAMP(layer == 0 ? 4 : 12); NPASS(layer == 0 ? 21 : 22, it);
      f32x4 acc[4];
#pragma unroll
      for (int nt = 0; nt < 4; ++nt) acc[nt] = mfma(oa[1], wo[1][nt], mfma(oa[0], wo[0][nt], f32x4{0.f, 0.f, 0.f, 0.f}));
      const float lg = __uint_as_float(lw);
      const float e = __builtin_amdgcn_exp2f(lg - xrow_max(lg));
      const float wt1 = e * __builtin_amdgcn_rcpf(xrow_sum(e));
      const float wt[4] = {dpp<0x00>(wt1), dpp<0x55>(wt1), dpp<0xAA>(wt1), dpp<0xFF>(wt1)};
#pragma unroll
      for (int t2 = 0; t2 < 2; ++t2) {
        float u[4];
#pragma unroll
        for (int r = 0; r < 4; ++r) u[r] = swap32_add(acc[t2][r] * wt[r], acc[t2 + 2][r] * wt[r]);
        const int n = 16 * (t2 + 2 * (grp >> 1)) + c16;
        *(LAS float*)(R + w * 1024 + (grp & 1) * 256 + n * 4) = swap16_add(u[0], u[1]);
        *(LAS float*)(R + w * 1024 + (2 + (grp & 1)) * 256 + n * 4) = swap16_add(u[2], u[3]);
      }
      BAR();
      if (w < 4) {
        float t[8];
#pragma unroll
        for (int v = 0; v < 8; ++v) t[v] = *(LAS float*)(R + v * 1024 + w * 256 + lane * 4);
        __builtin_amdgcn_sched_barrier(0);
        publish_pairs(rs, myout + 1024 + layer * 512 + w * 128, lane, ((t[0] + t[1]) + (t[2] + t[3])) + ((t[4] + t[5]) + (t[6] + t[7])));
        STAMP(layer == 0 ? 5 : 13);
      }
    }
  }
  if (qid == 0 && w == 0 && lane == 0) st8(rs, WS_EPOCH, v2u{ctl[2], ctl[3]});
  STAMP_DUMP();
}

__device__ __forceinline__ void f1_role(const Params& p, LAS unsigned char* L, unsigned lbase, __amdgpu_buffer_rsrc_t rs, int f, int w, int lane) {
  volatile LAS unsigned* ctl = (volatile LAS unsigned*)(L + L_CTL);
  const int c16 = lane & 15, grp = lane >> 4, b4 = lane & 3;
  const int myout = WS_H + f * H_STRIDE;
  if (w == 8) {
    control_announce(L, rs, lane, f);
    STAMP(2);
    STAMP_DUMP();
    return;
  }
  preset_chunk(rs, f, w * 64 + lane);
  if (f >= 28) return;
  STAMP(0);
  const int xcol = 32 * (2 * w + (grp >> 1)) + 8 * (c16 >> 2) + 4 * (grp & 1);
  v2u xr = *(const GAS v2u*)(p.x + b4 * 512 + xcol);
  pace<DLY_W1>();
  wslice_dma(p.w1, 3584, 64 * w, 128 * f, lbase + L_WA + w * 8192, lane);
  WAIT_VM(0);
  v4u w1f[2][4];
  wslice_frags(L + L_WA + w * 8192, lane, w1f);
  colsum_partial(L + L_RED, w, lane, w1f);
  BAR();
  float cs = 0.f;
  if (w < 4) cs = colsum_final(L + L_RED, lane);
  v4u sel;
  {
    const int r = c16 & 7;
    const unsigned v = (r & 1) ? 0x3F800000u : 0x00003F80u;
    sel = v4u{(r >> 1) == 0 ? v : 0u, (r >> 1) == 1 ? v : 0u, (r >> 1) == 2 ? v : 0u, (r >> 1) == 3 ? v : 0u};
  }
  STAMP(2);
  ctl_wait(ctl, 2u);
  STAMP(3);
#pragma unroll LAYER_UNROLL
  for (int layer = 0; layer < 2; ++layer) {
    LAS unsigned char* R = L + L_RED + (1 - layer) * RED_BUF;
    if (layer == 1) {
      pace<DLY_X1>();
      int it = 0;
      _Pragma("nounroll") for (; it < SPIN_CAP; ++it) {
        FRESH();
        xr = ld8(rs, WS_X2 + (xcol >> 4) * 128 + b4 * 32 + (xcol & 15) * 2);
        if (all_lanes(max(xr.x, xr.y) != SENT)) break;
        __builtin_amdgcn_s_sleep(1);
      }
      STAMP(10); NPASS(20, it);
      pace<DLY_C1>();
    } else {
      pace<DLY_C0>();
    }
    {
      const f32x4 xc = {bflo(xr.x), bfhi(xr.x), bflo(xr.y), bfhi(xr.y)};
      v4u pc[2][2];
      if (layer == 1) STAMP(18);
      int it = 0;
      _Pragma("nounroll") for (; it < SPIN_CAP; ++it) {
        FRESH();
#pragma unroll
        for (int u = 0; u < 2; ++u)
#pragma unroll
          for (int hf = 0; hf < 2; ++hf)
            pc[u][hf] = ld16(rs, WS_QO + ((grp + 4 * hf) * 8 + w) * QO_STRIDE + 1024 + layer * 512 + b4 * 128 + u * 64 + (c16 >> 2) * 16);
        if (all_lanes(max(max(mx4(pc[0][0]), mx4(pc[0][1])), max(mx4(pc[1][0]), mx4(pc[1][1]))) != SENT)) break;
        if (layer == 0) __builtin_amdgcn_s_sleep(4); else BACKOFF();
      }
      STAMP(layer == 0 ? 6 : 14); NPASS(layer == 0 ? 21 : 22, it);
      f32x4 s0 = mfma(sel, pc[0][0], xc), s1 = mfma(sel, pc[1][0], xc);
      s0 = mfma(sel, pc[0][1], s0);
      s1 = mfma(sel, pc[1][1], s1);
      const f32x4 sx = (grp < 2) ? s0 : s1;
      *(LAS v2u*)(L + L_ACT + b4 * ACT_ROW + xcol * 2) = v2u{pk(sx[0], sx[1]), pk(sx[2], sx[3])};
    }
    BAR();
    {
      v4u xa[2];
#pragma unroll
      for (int ks = 0; ks < 2; ++ks) xa[ks] = *(LAS v4u*)(L + L_ACT + (c16 >> 2) * ACT_ROW + (64 * w + 32 * ks + 8 * grp) * 2);
      ln_proj_partial(R, w, lane, xa, w1f);
    }
    BAR();
    if (w < 4) {
      float h = ln_proj_final(R, w, lane, cs);
      h = bflo(pk(h, h));
      publish_pairs(rs, myout + layer * 512 + w * 128, lane, h * __builtin_amdgcn_rcpf(1.0f + __expf(-h)));
      STAMP(layer == 0 ? 7 : 15);
    }
  }
  STAMP_DUMP();
}

__device__ __forceinline__ void f2_role(const Params& p, LAS unsigned char* L, unsigned lbase, __amdgpu_buffer_rsrc_t rs, int p2, int w, int lane) {
  volatile LAS unsigned* ctl = (volatile LAS unsigned*)(L + L_CTL);
  const int c16 = lane & 15, grp = lane >> 4;
  const int myout = WS_X2 + p2 * 128;
  if (w == 8) {
    control_announce(L, rs, lane, 32 + p2);
    STAMP(2);
    STAMP_DUMP();
    return;
  }
  preset_chunk(rs, 32 + p2, w * 64 + lane);
  STAMP(0);
  const int rb_ = (lane >> 1) & 3, rh = lane & 1;
  v4u xin = {0u, 0u, 0u, 0u};
  if (w == 1) xin = *(const GAS v4u*)(p.x + rb_ * 512 + 16 * p2 + 8 * rh);
  pace<DLY_W2>();
#pragma unroll
  for (int i = 0; i < 7; ++i) {
    const int rp = lane >> 1, r = rp ^ (((rp >> 3) & 1) << 2);
    dma16(p.w2, (unsigned)((224 * w + 32 * i + r) * 1024 + 32 * p2 + (lane & 1) * 16), lbase + L_WA + w * 8192 + i * 1024);
  }
  WAIT_VM(0);
  v4u w2f[7];
  {
    const int q = c16 >> 2, pp = lane & 3;
#pragma unroll
    for (int i = 0; i < 7; ++i) {
      v2u h[2];
#pragma unroll
      for (int t = 0; t < 2; ++t) {
        const int r = 8 * grp + 4 * t + q, rp = r ^ (((r >> 3) & 1) << 2);
        h[t] = trread(L + L_WA + w * 8192 + i * 1024 + rp * 32 + pp * 8);
      }
      w2f[i] = v4u{h[0].x, h[0].y, h[1].x, h[1].y};
    }
  }
  STAMP(2);
  ctl_wait(ctl, 2u);
  STAMP(3);
  float xs[8];
#pragma unroll
  for (int i = 0; i < 4; ++i) { xs[2 * i] = bflo(xin[i]); xs[2 * i + 1] = bfhi(xin[i]); }
#pragma unroll LAYER_UNROLL
  for (int layer = 0; layer < 2; ++layer) {
    LAS unsigned char* R = L + L_RED + layer * RED_BUF;
    LAS unsigned char* X1 = L + L_ACT + layer * 256;
    if (w == 1) {
      if (layer == 1) pace<DLY_FC1>();
      v4u pc;
      int it = 0;
      _Pragma("nounroll") for (; it < SPIN_CAP; ++it) {
        FRESH();
        pc = ld16(rs, WS_QO + ((lane >> 3) * 8 + (p2 >> 2)) * QO_STRIDE + 1024 + layer * 512 + rb_ * 128 + (p2 & 3) * 32 + rh * 16);
        if (all_lanes(mx4(pc) != SENT)) break;
        if (layer == 0) __builtin_amdgcn_s_sleep(3); else __builtin_amdgcn_s_sleep(1);
      }
      if (lane == 0) ctl[1] = (unsigned)(layer + 1);
      STAMP(layer == 0 ? 6 : 14); NPASS(layer == 0 ? 20 : 23, it);
      float v[8];
#pragma unroll
      for (int i = 0; i < 4; ++i) { v[2 * i] = bflo(pc[i]); v[2 * i + 1] = bfhi(pc[i]); }
#pragma unroll
      for (int i = 0; i < 8; ++i) { float t = v[i] + dpp<ROR8>(v[i]); t = xrow_sum(t); const unsigned r2 = pk(xs[i] + t, 0.f); xs[i] = bflo(r2); }
      if (lane < 8) {
        *(LAS f32x4*)(X1 + rb_ * 64 + rh * 32) = f32x4{xs[0], xs[1], xs[2], xs[3]};
        *(LAS f32x4*)(X1 + rb_ * 64 + rh * 32 + 16) = f32x4{xs[4], xs[5], xs[6], xs[7]};
      }
      pace<DLY_D1>();
    } else {
      ctl_wait(ctl + 1, (unsigned)(layer + 1));
      pace<DLY_D0>();
    }
    unsigned d[7];
    if (layer == 1) STAMP(18);
    int it = 0;
    _Pragma("nounroll") for (; it < SPIN_CAP; ++it) {
      FRESH();
      unsigned m = 0;
#pragma unroll
      for (int i = 0; i < 7; ++i) {
        const int k0 = 224 * w + 32 * i;
        d[i] = ld4(rs, WS_H + (k0 >> 6) * H_STRIDE + layer * 512 + (c16 >> 2) * 128 + ((k0 & 63) + 8 * grp + 2 * (c16 & 3)) * 2);
      }
#pragma unroll
      for (int i = 0; i < 7; ++i) m = max(m, d[i]);
      if (all_lanes(m != SENT)) break;
      BACKOFF();
    }
    STAMP(layer == 0 ? 8 : 16); NPASS(layer == 0 ? 21 : 22, it);
    f32x4 a0 = {0.f, 0.f, 0.f, 0.f}, a1 = a0;
#pragma unroll
    for (int i = 0; i < 7; ++i) { const v4u ha = quad_frag(d[i]); if (i & 1) a1 = mfma(ha, w2f[i], a1); else a0 = mfma(ha, w2f[i], a0); }
    *(LAS float*)(R + w * 256 + grp * 64 + c16 * 4) = a0[0] + a1[0];
    BAR();
    if (w == 0) {
      float t[8];
#pragma unroll
      for (int v = 0; v < 8; ++v) t[v] = *(LAS float*)(R + v * 256 + lane * 4);
      const float xr = *(LAS float*)(X1 + lane * 4);
      __builtin_amdgcn_sched_barrier(0);
      const float y = xr + (((t[0] + t[1]) + (t[2] + t[3])) + ((t[4] + t[5]) + (t[6] + t[7])));
      const unsigned o = pk(y, dpp<XOR1>(y));
      if ((lane & 1) == 0) {
        if (layer == 0) { st4(rs, myout + lane * 2, canon(o)); *(LAS unsigned*)(L + L_ACT + 1024 + lane * 2) = o; }
        else p.out[(lane >> 4) * 256 + 8 * p2 + ((lane & 15) >> 1)] = o;
      }
      STAMP(layer == 0 ? 9 : 17);
    }
    if (layer == 0) {
      BAR();
      if (w == 1) {
        const v4u x2 = *(LAS v4u*)(L + L_ACT + 1024 + rb_ * 32 + rh * 16);
#pragma unroll
        for (int i = 0; i < 4; ++i) { xs[2 * i] = bflo(x2[i]); xs[2 * i + 1] = bfhi(x2[i]); }
      }
    }
  }
  STAMP_DUMP();
}

__device__ __forceinline__ void attn_role(const Params& p, LAS unsigned char* L, unsigned lbase, __amdgpu_buffer_rsrc_t rs, int g, int idx, int w, int lane) {
  volatile LAS unsigned* ctl = (volatile LAS unsigned*)(L + L_CTL);
  const int c16 = lane & 15, grp = lane >> 4;
  const int b = idx >> 2, s = idx & 3;
  const int aid = g * 16 + idx;
  const int myout = WS_PO + aid * PO_STRIDE;

  if (w == 8) {
    const v2u tg = read_tag(rs);
    BAR();
    wait_preset(rs, lane, tg);
    STAMP(2);
    v4u q;
    _Pragma("nounroll") for (int it = 0; it < SPIN_CAP; ++it) {
      FRESH();
      q = ld16(rs, WS_QO + (g * 8 + (lane >> 3)) * QO_STRIDE + b * 128 + (lane & 7) * 16);
      if (all_lanes(mx4(q) != SENT)) break;
      BACKOFF();
    }
    *(LAS v4u*)(L + L_QL + (lane >> 4) * 320 + (lane & 15) * 16) = q;
    WAIT_LGKM0();
    if (lane == 0) ctl[0] = 3u;
    STAMP(3);
    STAMP_DUMP();
    return;
  }
  BAR();
  STAMP(0);
  const size_t kvoff = ((size_t)(b * 8 + g) * 4096 + 3072 + 256 * s + 32 * w) * 256;
  const unsigned char* kb = p.kc + kvoff;
  const unsigned char* vb = p.vc + kvoff;
#pragma unroll
  for (int i = 0; i < 8; ++i) {
    const int row = 4 * i + grp;
    dma16(kb, (unsigned)(row * 256 + ((c16 ^ (row & 15)) * 16)), lbase + L_K + w * 8192 + i * 1024);
  }
#pragma unroll
  for (int i = 0; i < 8; ++i) {
    const int row = 4 * i + grp;
    WAIT_VM(7);
    dma16(vb, (unsigned)(row * 256 + ((c16 ^ ((row & 7) << 1)) * 16)), lbase + L_V + w * 8192 + i * 1024);
  }
  v4u kf[2][4], vf[8], qf[4];
#pragma unroll
  for (int t = 0; t < 2; ++t)
#pragma unroll
    for (int st = 0; st < 4; ++st)
      kf[t][st] = *(LAS v4u*)(L + L_K + w * 8192 + (16 * t + c16) * 256 + (((4 * st + grp) ^ c16) * 16));
  STAMP(1);
  ctl_wait(ctl, 3u);
  const int h = c16 >> 2;
#pragma unroll
  for (int st = 0; st < 4; ++st) qf[st] = *(LAS v4u*)(L + L_QL + h * 320 + st * 64 + grp * 16);
  constexpr float CS = 0.08838834764831845f * 1.4426950408889634f;

#pragma unroll LAYER_UNROLL
  for (int layer = 0; layer < 2; ++layer) {
    if (layer == 1) {
      pace<DLY_A1>();
      unsigned d[4];
      int it = 0;
      _Pragma("nounroll") for (; it < SPIN_CAP; ++it) {
        FRESH();
#pragma unroll
        for (int st = 0; st < 4; ++st)
          d[st] = ld4(rs, WS_QO + (g * 8 + 2 * h + (st >> 1)) * QO_STRIDE + 512 + b * 128 + (32 * (st & 1) + 8 * grp + 2 * (c16 & 3)) * 2);
        if (all_lanes(max(max(d[0], d[1]), max(d[2], d[3])) != SENT)) break;
        __builtin_amdgcn_s_sleep(1);
      }
      NPASS(28, it);
#pragma unroll
      for (int st = 0; st < 4; ++st) qf[st] = quad_frag(d[st]);
    }
    STAMP(layer == 0 ? 2 : 5);
    f32x4 sc[2];
#pragma unroll
    for (int t = 0; t < 2; ++t) {
      f32x4 a = {0.f, 0.f, 0.f, 0.f};
#pragma unroll
      for (int st = 0; st < 4; ++st) a = mfma(kf[t][st], qf[st], a);
      sc[t] = a;
    }
    float m = fmx(fmx(fmx(sc[0][0], sc[0][1]), fmx(sc[0][2], sc[0][3])), fmx(fmx(sc[1][0], sc[1][1]), fmx(sc[1][2], sc[1][3])));
    FSTAMP(20);
    m = xrow_max(m) * CS;
    const bool q1 = lane & 1, q2 = lane & 2;
    const float s0 = q2 ? (q1 ? sc[1][2] : sc[1][0]) : (q1 ? sc[0][2] : sc[0][0]);
    const float s1 = q2 ? (q1 ? sc[1][3] : sc[1][1]) : (q1 ? sc[0][3] : sc[0][1]);
    const float p0 = __builtin_amdgcn_exp2f(fmaf(s0, CS, -m)), p1 = __builtin_amdgcn_exp2f(fmaf(s1, CS, -m));
    *(LAS unsigned*)(L + L_PL + w * 256 + lane * 4) = pk(p0, p1);
    float l = p0 + p1;
    l += dpp<XOR1>(l);
    l += dpp<XOR2>(l);
    l = xrow_sum(l);
    if (lane < 16 && (c16 & 3) == 0) *(LAS f32x2*)(L + L_ST + h * 64 + w * 8) = f32x2{m, l};
    if (layer == 0) WAIT_VM(0);
    FSTAMP(21);
    BAR();
    FSTAMP(22);
    if (layer == 0) {
      const int q4 = c16 >> 2, pp = lane & 3;
#pragma unroll
      for (int v = 0; v < 8; ++v) {
        const int r0 = 4 * grp + q4;
        const int sl = ((2 * w + (pp >> 1)) ^ ((r0 & 7) << 1)) * 16 + (pp & 1) * 8;
        LAS unsigned char* a = L + L_V + v * 8192 + r0 * 256 + sl;
        vf[v] = trfrag(a, a + 4096);
      }
      STAMP(3);
    }
    v4u pv[8];
#pragma unroll
    for (int v = 0; v < 8; ++v) pv[v] = *(LAS v4u*)(L + L_PL + (v * 16 + grp * 4 + h) * 16);
    f32x4 ml[4];
#pragma unroll
    for (int i = 0; i < 4; ++i) ml[i] = *(LAS f32x4*)(L + L_ST + h * 64 + i * 16);
    __builtin_amdgcn_sched_barrier(0);
    f32x4 o[8];
#pragma unroll
    for (int v = 0; v < 8; ++v) o[v] = mfma(vf[v], pv[v], f32x4{0.f, 0.f, 0.f, 0.f});
    const float M = fmx(fmx(fmx(ml[0][0], ml[0][2]), fmx(ml[1][0], ml[1][2])), fmx(fmx(ml[2][0], ml[2][2]), fmx(ml[3][0], ml[3][2])));
    FSTAMP(23);
    float Ls = 0.f;
    f32x4 acc = {0.f, 0.f, 0.f, 0.f};
#pragma unroll
    for (int v = 0; v < 8; ++v) {
      const float f = __builtin_amdgcn_exp2f(ml[v >> 1][2 * (v & 1)] - M);
      Ls = fmaf(f, ml[v >> 1][2 * (v & 1) + 1], Ls);
      acc += f * o[v];
    }
    const float inv = __builtin_amdgcn_rcpf(Ls);
    FSTAMP(24);
    if ((c16 & 3) == 0) st8(rs, myout + layer * PO_LAYER + w * 128 + h * 32 + grp * 8, v2u{canon(pk(acc[0] * inv, acc[1] * inv)), canon(pk(acc[2] * inv, acc[3] * inv))});
    if (w == 0 && lane < 16 && (c16 & 3) == 0) st4(rs, myout + layer * PO_LAYER + 1024 + h * 4, canon(__float_as_uint(M + __builtin_amdgcn_logf(Ls))));
    STAMP(layer == 0 ? 4 : 6);
  }
  STAMP_DUMP();
}

#define CODE_KB 20
__global__ void __launch_bounds__(NTHR) fused_layers_kernel(Params p) {
  extern __shared__ __attribute__((aligned(16))) unsigned char smem[];
  LAS unsigned char* L = (LAS unsigned char*)smem;
  const unsigned lbase = (unsigned)(uintptr_t)L;
  const int tid = threadIdx.x, lane = tid & 63, w = __builtin_amdgcn_readfirstlane(tid >> 6);
  if (CODE_KB > 0 && w < 8 && blockIdx.x < 192) {
    const u64 pc = __builtin_amdgcn_s_getpc() & ~255ull;
    unsigned keep;
#pragma unroll
    for (int i = 0; i < (CODE_KB + 7) / 8; ++i) {
      const int kb = min(w + 8 * i, CODE_KB - 1);
      asm volatile("s_mov_b32 %0, m0\n\ts_mov_b32 m0, %3\n\ts_nop 0\n\tglobal_load_lds_dwordx4 %1, %2\n\ts_mov_b32 m0, %0"
                   : "=&s"(keep) : "v"(kb * 1024 + lane * 16), "s"(pc), "s"(lbase + L_SCR) : "memory");
    }
  }
  const int g = blockIdx.x & 7, idx = blockIdx.x >> 3;
  const __amdgpu_buffer_rsrc_t rs = __builtin_amdgcn_make_buffer_rsrc((void*)p.ws, 0, WS_END, 0x00020000);
  if (tid == 512) { *(volatile LAS unsigned*)(L + L_CTL) = 0u; *(volatile LAS unsigned*)(L + L_CTL + 4) = 0u; }
  if (idx < 16) attn_role(p, L, lbase, rs, g, idx, w, lane);
  else if (idx < 24) qo_role(p, L, lbase, rs, g, idx - 16, w, lane);
  else if (idx < 28) f1_role(p, L, lbase, rs, g * 4 + (idx - 24), w, lane);
  else f2_role(p, L, lbase, rs, g * 4 + (idx - 28), w, lane);
}

}

extern "C" void kernel_launch(void* const* d_in, const int* in_sizes, int n_in, void* d_out, int out_size, void* d_ws, size_t ws_size,
                              hipStream_t stream) {
  (void)in_sizes; (void)n_in; (void)out_size; (void)ws_size;
  Params p;
  p.x = (const unsigned short*)d_in[0];
  p.kc = (const unsigned char*)d_in[1];
  p.vc = (const unsigned char*)d_in[2];
  p.wq = (const unsigned char*)d_in[3];
  p.wo = (const unsigned char*)d_in[4];
  p.w1 = (const unsigned char*)d_in[6];
  p.w2 = (const unsigned char*)d_in[7];
  p.out = (unsigned*)d_out;
  p.ws = (unsigned char*)d_ws;
  (void)hipFuncSetAttribute((const void*)fused_layers_kernel, hipFuncAttributeMaxDynamicSharedMemorySize, LDS_BYTES);
  hipLaunchKernelGGL(fused_layers_kernel, dim3(NWG), dim3(NTHR), LDS_BYTES, stream, p);
}
```

```cpp
#include <hip/hip_runtime.h>
#include <stdint.h>

#define LAS __attribute__((address_space(3)))
#define GAS __attribute__((address_space(1)))
typedef unsigned v4u __attribute__((ext_vector_type(4)));
typedef unsigned v2u __attribute__((ext_vector_type(2)));
typedef float f32x4 __attribute__((ext_vector_type(4)));
typedef float f32x2 __attribute__((ext_vector_type(2)));
typedef short bf16x8 __attribute__((ext_vector_type(8)));
typedef short s16x4 __attribute__((ext_vector_type(4)));
typedef __bf16 bf16x2 __attribute__((ext_vector_type(2)));
typedef unsigned long long u64;

namespace {

constexpr int NWG = 256, NTHR = 576;
constexpr unsigned SENT = 0xFFFFFFFFu;
constexpr int SPIN_CAP = 1 << 20;
constexpr int AUX_SC1 = 16;

constexpr int WS_EPOCH = 0;
constexpr int WS_FL = 1024;
constexpr int HB0 = 0, HB1 = 1, HC0 = 2, HD0 = 3;
constexpr int CP = 8192;
constexpr int WS_QO = 16384;
constexpr int WS_H = WS_QO + 16 * 16384;
constexpr int WS_X2 = WS_H + 4 * 16384;
constexpr int WS_PO = WS_X2 + 16384;
constexpr int WS_PS = WS_PO + 32 * 16384;
constexpr int WS_SLOTS_END = WS_PS + 4 * 16384;
constexpr int CLR_CHUNK = (WS_SLOTS_END - WS_QO) / 64;
__device__ __forceinline__ int qo_slot(int q) { return WS_QO + (q >> 2) * 16384 + (q & 3) * 2048; }
__device__ __forceinline__ int h_slot(int f) { return WS_H + (f >> 3) * 16384 + (f & 7) * 1024; }
__device__ __forceinline__ int po_slot(int a) { return WS_PO + (a >> 2) * 16384 + (a & 3) * 2048; }
__device__ __forceinline__ int ps_slot(int a) { return WS_PS + (a >> 5) * 16384 + (a & 31) * 256; }
constexpr int WS_Q0 = WS_SLOTS_END;
constexpr int WS_HINT = (WS_Q0 + 64 * 2048 + 16383) & ~16383;
constexpr int HN = 4;
constexpr int WS_PROBE = WS_HINT + 16384;
constexpr int WS_END = WS_PROBE + 16384;
static_assert(CLR_CHUNK % 128 == 0 && CLR_CHUNK / 16 <= 1024, "preset chunks");

constexpr int L_WA = 0, L_WB = 65536;
constexpr int L_K = 0, L_V = 65536;
constexpr int L_QL = 131072, L_PL = L_QL + 1280, L_ST = L_PL + 2048;
constexpr int L_RED = 131072, RED_BUF = 10496, RED_CS = 8192, RED_G = 10240;
constexpr int L_ACT = L_RED + 2 * RED_BUF, ACT_ROW = 1088;
constexpr int L_CTL = L_ACT + 4 * ACT_ROW;
constexpr int L_SCR = L_CTL + 64;
constexpr int LDS_BYTES = L_SCR + 1024 + 16;
static_assert(LDS_BYTES <= 160 * 1024, "LDS map");

struct Params {
  const unsigned short* x;
  const unsigned char* kc;
  const unsigned char* vc;
  const unsigned char* wq;
  const unsigned char* wo;
  const unsigned char* w1;
  const unsigned char* w2;
  unsigned* out;
  unsigned char* ws;
};

__device__ __forceinline__ f32x4 mfma(v4u a, v4u b, f32x4 c) {
  return __builtin_amdgcn_mfma_f32_16x16x32_bf16(__builtin_bit_cast(bf16x8, a), __builtin_bit_cast(bf16x8, b), c, 0, 0, 0);
}
__device__ __forceinline__ unsigned pk(float a, float b) {
  bf16x2 v = {(__bf16)a, (__bf16)b};
  return __builtin_bit_cast(unsigned, v);
}
__device__ __forceinline__ float dot2(unsigned a, unsigned b, float c) {
  return __builtin_amdgcn_fdot2_f32_bf16(__builtin_bit_cast(bf16x2, a), __builtin_bit_cast(bf16x2, b), c, false);
}
__device__ __forceinline__ unsigned canon(unsigned u) { return u == SENT ? 0x7FC07FC0u : u; }
__device__ __forceinline__ float bflo(unsigned u) { return __uint_as_float(u << 16); }
__device__ __forceinline__ float bfhi(unsigned u) { return __uint_as_float(u & 0xffff0000u); }
__device__ __forceinline__ unsigned mx4(v4u x) { return max(max(x.x, x.y), max(x.z, x.w)); }
__device__ __forceinline__ bool all_lanes(bool ok) { return __builtin_amdgcn_ballot_w64(!ok) == 0ull; }
template <int CTRL> __device__ __forceinline__ unsigned dppu(unsigned x) { return (unsigned)__builtin_amdgcn_mov_dpp((int)x, CTRL, 0xf, 0xf, true); }
template <int CTRL> __device__ __forceinline__ float dpp(float x) { return __uint_as_float(dppu<CTRL>(__float_as_uint(x))); }
constexpr int ROR4 = 0x124, ROR8 = 0x128, XOR1 = 0xB1, XOR2 = 0x4E, HMIR = 0x141;
__device__ __forceinline__ float fmx(float a, float b) { return __builtin_elementwise_maximum(a, b); }
__device__ __forceinline__ v4u quad_frag(unsigned d) { return v4u{dppu<0x00>(d), dppu<0x55>(d), dppu<0xAA>(d), dppu<0xFF>(d)}; }
__device__ __forceinline__ float xrow_max(float x) {
  auto s = __builtin_amdgcn_permlane16_swap(__float_as_uint(x), __float_as_uint(x), false, false);
  x = fmx(__uint_as_float(s[0]), __uint_as_float(s[1]));
  auto t = __builtin_amdgcn_permlane32_swap(__float_as_uint(x), __float_as_uint(x), false, false);
  return fmx(__uint_as_float(t[0]), __uint_as_float(t[1]));
}
__device__ __forceinline__ float xrow_sum(float x) {
  auto s = __builtin_amdgcn_permlane16_swap(__float_as_uint(x), __float_as_uint(x), false, false);
  x = __uint_as_float(s[0]) + __uint_as_float(s[1]);
  auto t = __builtin_amdgcn_permlane32_swap(__float_as_uint(x), __float_as_uint(x), false, false);
  return __uint_as_float(t[0]) + __uint_as_float(t[1]);
}
__device__ __forceinline__ float swap32_add(float a, float b) {
  auto t = __builtin_amdgcn_permlane32_swap(__float_as_uint(a), __float_as_uint(b), false, false);
  return __uint_as_float(t[0]) + __uint_as_float(t[1]);
}
__device__ __forceinline__ float swap16_add(float a, float b) {
  auto t = __builtin_amdgcn_permlane16_swap(__float_as_uint(a), __float_as_uint(b), false, false);
  return __uint_as_float(t[0]) + __uint_as_float(t[1]);
}
__device__ __forceinline__ void dma16(const void* gbase, unsigned voff, unsigned dst) {
  unsigned keep;
  asm volatile("s_mov_b32 %0, m0\n\ts_mov_b32 m0, %3\n\ts_nop 0\n\tglobal_load_lds_dwordx4 %1, %2 nt\n\ts_mov_b32 m0, %0"
               : "=&s"(keep) : "v"(voff), "s"(gbase), "s"(dst) : "memory");
}
__device__ __forceinline__ void warm16nt(const void* gbase, unsigned voff, unsigned dst) {
  unsigned keep;
  asm volatile("s_mov_b32 %0, m0\n\ts_mov_b32 m0, %3\n\ts_nop 0\n\tglobal_load_lds_dwordx4 %1, %2 nt\n\ts_mov_b32 m0, %0"
               : "=&s"(keep) : "v"(voff), "s"(gbase), "s"(dst) : "memory");
}
__device__ __forceinline__ void warm16(const void* gbase, unsigned voff, unsigned dst) {
  unsigned keep;
  asm volatile("s_mov_b32 %0, m0\n\ts_mov_b32 m0, %3\n\ts_nop 0\n\tglobal_load_lds_dwordx4 %1, %2\n\ts_mov_b32 m0, %0"
               : "=&s"(keep) : "v"(voff), "s"(gbase), "s"(dst) : "memory");
}
__device__ __forceinline__ v2u trread(LAS unsigned char* p) {
  return __builtin_bit_cast(v2u, __builtin_amdgcn_ds_read_tr16_b64_v4i16((LAS s16x4*)p));
}
__device__ __forceinline__ v4u trfrag(LAS unsigned char* p0, LAS unsigned char* p1) {
  const v2u a = trread(p0), b = trread(p1);
  return v4u{a.x, a.y, b.x, b.y};
}
#define WAIT_VM(n) asm volatile("s_waitcnt vmcnt(" #n ")" ::: "memory")
#define WAIT_LGKM0() asm volatile("s_waitcnt lgkmcnt(0)" ::: "memory")
#define BAR() do { WAIT_LGKM0(); __builtin_amdgcn_s_barrier(); asm volatile("" ::: "memory"); } while (0)
#define FRESH() asm volatile("" ::: "memory")

__device__ __forceinline__ v4u ld16(__amdgpu_buffer_rsrc_t rs, int off) { return __builtin_amdgcn_raw_buffer_load_b128(rs, off, 0, AUX_SC1); }
__device__ __forceinline__ v2u ld8(__amdgpu_buffer_rsrc_t rs, int off) { return __builtin_amdgcn_raw_buffer_load_b64(rs, off, 0, AUX_SC1); }
__device__ __forceinline__ unsigned ld4(__amdgpu_buffer_rsrc_t rs, int off) { return __builtin_amdgcn_raw_buffer_load_b32(rs, off, 0, AUX_SC1); }
__device__ __forceinline__ void st16(__amdgpu_buffer_rsrc_t rs, int off, v4u v) { __builtin_amdgcn_raw_buffer_store_b128(v, rs, off, 0, AUX_SC1); }
__device__ __forceinline__ void st8(__amdgpu_buffer_rsrc_t rs, int off, v2u v) { __builtin_amdgcn_raw_buffer_store_b64(v, rs, off, 0, AUX_SC1); }
__device__ __forceinline__ void st4(__amdgpu_buffer_rsrc_t rs, int off, unsigned v) { __builtin_amdgcn_raw_buffer_store_b32(v, rs, off, 0, AUX_SC1); }

__device__ __forceinline__ void ctl_wait(volatile LAS unsigned* ctl, unsigned v) {
  _Pragma("nounroll") for (int i = 0; i < SPIN_CAP * 4; ++i) { if (*ctl >= v) break; __builtin_amdgcn_s_sleep(1); }
  asm volatile("" ::: "memory");
}
template <int N> __device__ __forceinline__ void pace() {
#pragma unroll
  for (int i = 0; i < N / 120; ++i) __builtin_amdgcn_s_sleep(120);
  if (N % 120) __builtin_amdgcn_s_sleep(N % 120);
}
#define BACKOFF() __builtin_amdgcn_s_sleep(POLL_SLEEP)
#define POLL2(res, a_, b_, GAP, LOAD, OK, it) do { \
    LOAD(a_); __builtin_amdgcn_s_sleep(GAP); \
    _Pragma("nounroll") for (; it < SPIN_CAP; ++it) { \
      FRESH(); LOAD(b_); if (all_lanes(OK(a_))) { res = a_; break; } \
      FRESH(); LOAD(a_); if (all_lanes(OK(b_))) { res = b_; break; } \
    } } while (0)
#define KEEP(x) asm volatile("" :: "v"(x))

#define LITE 0
#define CY(i) do {} while (0)
#define STAMP(i) do {} while (0)
#define NPASS(i, n) do {} while (0)
#define FSTAMP(i) do {} while (0)
#define CLK(i, t) do {} while (0)
#define STAMP_DUMP() do {} while (0)

#define LAYER_UNROLL 2
#define POLL_SLEEP 1
#define DLY_P 36
#define DLY_K 8
#define DLY_WO 35
#define DLY_W1 145
#define DLY_W2 145
#define PROBE_N 2
#define DLY_M 10
#define OUT_SC1 1
#define TOUCH 0
#define KV_DMA 0
#define WARM_K 0
#define WARM_K_NT 1
#define WARM_Q 1
#define WARM_O 1
#define DLY_WOW 104
#define W2_WARM 1
#define DLY_W2B 8
#define DLY_HQ 28
#define DLY_HA 60
#define B0_POLL2 1
#define GAP_B 4
#define GAP_E 2
#define GAP_A 2
#define GAP_D 3
#define T_Q0 340
#define D_C0 54
#define D_C1 46
#define D_D0 93
#define D_D1 109
#define D_E 164
#define D_X 240
#define D_A 126
#define D_B 163

#define TP(name) name
#define PACE(name) pace<name>()
#define PACE_DIFF(a, b) pace<a - b>()
#define TUNE_ARG
#define TUNE_PASS
__device__ __forceinline__ unsigned clock32() { return (unsigned)__builtin_amdgcn_s_memrealtime(); }
__device__ __forceinline__ unsigned clock32_now() { const unsigned t = clock32(); __builtin_amdgcn_s_waitcnt(0xC07F); return t; }
__device__ __forceinline__ void wait_clock(unsigned t) {
  _Pragma("nounroll") for (int i = 0; i < 100000; ++i) {
    const int d = (int)(t - clock32());
    if (d <= 0) break;
    if (d > 12) __builtin_amdgcn_s_sleep(2);
  }
}
__device__ __forceinline__ unsigned fetch_hint(volatile LAS unsigned* dst, __amdgpu_buffer_rsrc_t rs, int lane, unsigned tag32, int h, int own, int nb) {
  v2u v;
  _Pragma("nounroll") for (int it = 0; it < SPIN_CAP; ++it) {
    FRESH();
    v = ld8(rs, WS_HINT + nb + h * 1024 + (own >= 0 ? own : (lane & 7)) * 128);
    if (all_lanes(v.y == tag32)) break;
    __builtin_amdgcn_s_sleep(1);
  }
  const unsigned t0 = __builtin_amdgcn_readfirstlane(v.x);
  unsigned t = t0;
  if (own < 0) {
    float d = (float)(int)(v.x - t0);
    d = fmx(d, dpp<XOR1>(d));
    d = fmx(d, dpp<XOR2>(d));
    d = fmx(d, dpp<HMIR>(d));
    t = t0 + (unsigned)(int)__builtin_amdgcn_readfirstlane((int)d);
  }
  t |= 1u;
  if (lane == 0) *dst = t;
  return t;
}
__device__ __forceinline__ unsigned lds_clock(volatile LAS unsigned* src) {
  _Pragma("nounroll") for (int i = 0; i < SPIN_CAP * 4; ++i) { if (*src != 0u) break; __builtin_amdgcn_s_sleep(1); }
  asm volatile("" ::: "memory");
  return __builtin_amdgcn_readfirstlane(*src);
}

__device__ __forceinline__ unsigned my_side() { return ((unsigned)__builtin_amdgcn_s_getreg(20 | (3 << 11)) >> 2) & 1u; }
__device__ __forceinline__ unsigned measure_orientation(__amdgpu_buffer_rsrc_t rs, int lane, int g, unsigned tag32) {
  const int base = WS_PROBE + g * 1024;
  st16(rs, base + (lane >> 5) * CP + ((lane >> 3) & 3) * 256 + (lane & 7) * 16, v4u{0u, 0u, 0u, 0u});
  WAIT_VM(0);
  unsigned best[2] = {~0u, ~0u};
#pragma unroll
  for (int k = 0; k < 2 * PROBE_N; ++k) {
    u64 t0, t1;
    asm volatile("s_memtime %0\n\ts_waitcnt lgkmcnt(0)" : "=s"(t0) :: "memory");
    unsigned v = ld4(rs, base + (k & 1) * CP + (k >> 1) * 256);
    asm volatile("s_waitcnt vmcnt(0)" : "+v"(v) :: "memory");
    asm volatile("s_memtime %0\n\ts_waitcnt lgkmcnt(0)" : "=s"(t1) :: "memory");
    best[k & 1] = min(best[k & 1], (unsigned)(t1 - t0));
  }
  const unsigned o = (best[1] < best[0] ? 1u : 0u) ^ my_side();
  if (lane < 8) st16(rs, WS_HINT + HN * 1024 + g * 128 + lane * 16, v4u{o, tag32, o, tag32});
  return best[0] | (best[1] << 16);
}
__device__ __forceinline__ int near_copy(__amdgpu_buffer_rsrc_t rs, int g, unsigned tag32, v2u h) {
  _Pragma("nounroll") for (int it = 0; it < SPIN_CAP; ++it) {
    if (__builtin_amdgcn_readfirstlane(h.y) == tag32) break;
    FRESH();
    h = ld8(rs, WS_HINT + HN * 1024 + g * 128);
  }
  return (int)(((__builtin_amdgcn_readfirstlane(h.x) ^ my_side()) & 1u) * CP);
}

__device__ __forceinline__ v2u read_tag(__amdgpu_buffer_rsrc_t rs) {
  const v2u e = ld8(rs, WS_EPOCH);
  const u64 tag = (((u64)e.y << 32) | e.x) + 1ull;
  return v2u{(unsigned)tag, (unsigned)(tag >> 32)};
}
__device__ __forceinline__ void wait_preset(__amdgpu_buffer_rsrc_t rs, int lane, v2u tg TUNE_ARG) {
  PACE(DLY_P);
  _Pragma("nounroll") for (int it = 0; it < SPIN_CAP; ++it) {
    FRESH();
    const v4u a = ld16(rs, WS_FL + (lane & 31) * 16);
    if (all_lanes(a.x == tg.x && a.y == tg.y && a.z == tg.x && a.w == tg.y)) break;
    BACKOFF();
  }
}
__device__ __forceinline__ void preset_chunk(__amdgpu_buffer_rsrc_t rs, int chunk, int tid) {
  st16(rs, WS_QO + chunk * CLR_CHUNK + tid * 16, v4u{SENT, SENT, SENT, SENT});
  if (tid + 512 < CLR_CHUNK / 16) st16(rs, WS_QO + chunk * CLR_CHUNK + (tid + 512) * 16, v4u{SENT, SENT, SENT, SENT});
  else if (tid >= 504) st16(rs, WS_HINT + (chunk >> 5) * CP + (chunk & 31) * 128 + (tid - 504) * 16, v4u{SENT, SENT, SENT, SENT});
  WAIT_VM(0);
  BAR();
}
__device__ __forceinline__ void control_announce(const Params& p, LAS unsigned char* L, unsigned lbase, __amdgpu_buffer_rsrc_t rs, int lane, int chunk, int g, int i TUNE_ARG) {
  volatile LAS unsigned* ctl = (volatile LAS unsigned*)(L + L_CTL);
  const v2u tg = read_tag(rs);
  BAR();
  if (lane == 0) { st8(rs, WS_FL + chunk * 8, tg); ctl[2] = tg.x; ctl[3] = tg.y; WAIT_LGKM0(); }
  if (WARM_Q) {
#pragma unroll
    for (int k = 0; k < 8; ++k) warm16(p.wq, (unsigned)((64 * i + 8 * k + (lane >> 3)) * 8192 + g * 1024 + 384 + (lane & 7) * 16), lbase + L_SCR);
  }
  if (i == 4) measure_orientation(rs, lane, g, tg.x);
  wait_preset(rs, lane, tg TUNE_PASS);
  if (lane == 0) ctl[0] = 2u;
}

__device__ __forceinline__ void wslice_dma(const unsigned char* W, unsigned rowbytes, unsigned row0, unsigned colbyte, unsigned dst, int lane) {
#pragma unroll
  for (int i = 0; i < 8; ++i) {
    const int r = 8 * i + (lane >> 3);
    const int sw = (((r >> 1) & 1) | (((r >> 3) & 1) << 1)) << 1;
    dma16(W, (row0 + r) * rowbytes + colbyte + (((lane & 7) ^ sw) << 4), dst + i * 1024);
  }
}
__device__ __forceinline__ void wslice_frags(LAS unsigned char* base, int lane, v4u (&f)[2][4]) {
  const int grp = lane >> 4, q = (lane & 15) >> 2, pp = lane & 3;
#pragma unroll
  for (int ks = 0; ks < 2; ++ks)
#pragma unroll
    for (int nt = 0; nt < 4; ++nt) {
      v2u h[2];
#pragma unroll
      for (int t = 0; t < 2; ++t) {
        const int r = 32 * ks + 8 * grp + 4 * t + q;
        const int sw = (((r >> 1) & 1) | (((r >> 3) & 1) << 1)) << 1;
        h[t] = trread(base + r * 128 + (((2 * nt + (pp >> 1)) ^ sw) << 4) + (pp & 1) * 8);
      }
      f[ks][nt] = v4u{h[0].x, h[0].y, h[1].x, h[1].y};
    }
}

__device__ __forceinline__ void proj_partial(LAS unsigned char* R, int w, int lane, const v4u (&xa)[2], const v4u (&wf)[2][4]) {
  const int c16 = lane & 15, grp = lane >> 4;
  const f32x4 z = {0.f, 0.f, 0.f, 0.f};
  f32x4 acc[4];
#pragma unroll
  for (int nt = 0; nt < 4; ++nt) acc[nt] = mfma(xa[1], wf[1][nt], mfma(xa[0], wf[0][nt], z));
#pragma unroll
  for (int nt = 0; nt < 4; ++nt) *(LAS float*)(R + w * 1024 + grp * 256 + (16 * nt + c16) * 4) = acc[nt][0];
}
__device__ __forceinline__ void colsum_partial(LAS unsigned char* R, int w, int lane, const v4u (&wf)[2][4]) {
  const v4u ones = {0x3F803F80u, 0x3F803F80u, 0x3F803F80u, 0x3F803F80u};
  const f32x4 z = {0.f, 0.f, 0.f, 0.f};
#pragma unroll
  for (int nt = 0; nt < 4; ++nt) {
    const f32x4 c = mfma(ones, wf[1][nt], mfma(ones, wf[0][nt], z));
    if (lane < 16) *(LAS float*)(R + RED_CS + w * 256 + (16 * nt + lane) * 4) = c[0];
  }
}
__device__ __forceinline__ float colsum_final(LAS unsigned char* R, int col) {
  float c = 0.f;
#pragma unroll
  for (int v = 0; v < 8; ++v) c += *(LAS float*)(R + RED_CS + v * 256 + col * 4);
  return c;
}
__device__ __forceinline__ float ln_apply(f32x2 g, float y, float cs) {
  g.x += dpp<XOR1>(g.x); g.y += dpp<XOR1>(g.y);
  g.x += dpp<XOR2>(g.x); g.y += dpp<XOR2>(g.y);
  g.x += dpp<HMIR>(g.x); g.y += dpp<HMIR>(g.y);
  const float mu = g.y * (1.0f / 512.0f);
  const float var = fmaxf(g.x * (1.0f / 512.0f) - mu * mu, 0.0f);
  const float rstd = __builtin_amdgcn_rsqf(var + 1e-5f);
  return rstd * (y - mu * cs);
}
__device__ __forceinline__ float ln_proj_final(LAS unsigned char* R, int b, int col, int lane, float cs) {
  float t[8];
#pragma unroll
  for (int v = 0; v < 8; ++v) t[v] = *(LAS float*)(R + v * 1024 + b * 256 + col * 4);
  f32x2 g = *(LAS f32x2*)(R + RED_G + (lane & 7) * 32 + b * 8);
  __builtin_amdgcn_sched_barrier(0);
  g.x += dpp<XOR1>(g.x); g.y += dpp<XOR1>(g.y);
  g.x += dpp<XOR2>(g.x); g.y += dpp<XOR2>(g.y);
  g.x += dpp<HMIR>(g.x); g.y += dpp<HMIR>(g.y);
  const float mu = g.y * (1.0f / 512.0f);
  const float var = fmaxf(g.x * (1.0f / 512.0f) - mu * mu, 0.0f);
  const float rstd = __builtin_amdgcn_rsqf(var + 1e-5f);
  const float y = ((t[0] + t[1]) + (t[2] + t[3])) + ((t[4] + t[5]) + (t[6] + t[7]));
  return rstd * (y - mu * cs);
}
__device__ __forceinline__ void publish_pairs(__amdgpu_buffer_rsrc_t rs, int off, int lane, float v) {
  const unsigned o = canon(pk(v, dpp<XOR1>(v)));
  if ((lane & 1) == 0) { st4(rs, off + lane * 2, o); st4(rs, off + CP + lane * 2, o); }
}

__device__ __forceinline__ void warm_k(const Params& p, unsigned lbase, int g, int i, int w, int lane) {
  if (WARM_K == 0) return;
#pragma unroll
  for (int q = 0; q < 2 * WARM_K; ++q)
#pragma unroll
    for (int a = 0; a < 2; ++a) {
      const int idx = 2 * i + a;
      const unsigned char* kb = p.kc + ((size_t)((idx >> 2) * 8 + g) * 4096 + 3072 + 256 * (idx & 3) + 32 * w) * 256;
      const unsigned off = (unsigned)((32 - 8 * WARM_K + 4 * q + (lane >> 4)) * 256 + (lane & 15) * 16);
      if (WARM_K_NT) warm16nt(kb, off, lbase + L_SCR); else warm16(kb, off, lbase + L_SCR);
    }
}

struct H2 { unsigned v[2]; };
struct G9 { unsigned v[9]; };
struct H4 { unsigned v[4]; };
__device__ __forceinline__ void warm_o(const Params& p, unsigned lbase, int g, int i, int w, int lane) {
  if (WARM_O == 1) warm16(p.wo, (unsigned)((512 * g + 64 * i + 8 * w + (lane >> 3)) * 1024 + 384 + (lane & 7) * 16), lbase + L_SCR);
  if (WARM_O == 2) {
    if (w < 4) warm16(p.wo, (unsigned)((512 * g + 32 * i + 8 * w + (lane >> 3)) * 1024 + 384 + (lane & 7) * 16), lbase + L_SCR);
#pragma unroll
    for (int k = 0; k < 4; ++k) warm16(p.wo, (unsigned)((512 * g + 256 + 32 * w + 8 * k + (lane >> 3)) * 1024 + 128 * i + (lane & 7) * 16), lbase + L_SCR);
  }
}
__device__ __forceinline__ void touch_pages(const Params& p, unsigned lbase, int g, int i, int w, int lane) {
  if (!TOUCH) return;
  const int n = w * 64 + lane;
  const unsigned char* a;
  if (i < 4) a = p.kc + ((size_t)(i * 8 + g) * 4096 + 3072) * 256 + (n & 63) * 4096;
  else if (i == 4) a = p.wo + (size_t)g * 512 * 1024 + (n & 127) * 4096;
  else if (i == 5) a = p.w1 + (size_t)min(n, 447) * 4096;
  else if (i == 6) a = p.w2 + (size_t)min(n, 447) * 4096;
  else a = p.vc + ((size_t)((n >> 6 & 3) * 8 + g) * 4096 + 3072) * 256 + (n & 63) * 4096;
  if (i < 4 && w > 0) return;
  if (i == 4 && w > 1) return;
  if (i == 7 && w > 3) return;
  unsigned keep;
  asm volatile("s_mov_b32 %0, m0\n\ts_mov_b32 m0, %2\n\ts_nop 0\n\tglobal_load_lds_dword %1, off nt\n\ts_mov_b32 m0, %0"
               : "=&s"(keep) : "v"(a), "s"(lbase + L_SCR) : "memory");
}

__device__ __forceinline__ void qo_role(const Params& p, LAS unsigned char* L, unsigned lbase, __amdgpu_buffer_rsrc_t rs, int g, int j, int w, int lane TUNE_ARG) {
  volatile LAS unsigned* ctl = (volatile LAS unsigned*)(L + L_CTL);
  const int c16 = lane & 15, grp = lane >> 4;
  const int qid = g * 8 + j;
  const int myout = qo_slot(qid);

  if (w == 8) {
    const v2u tg = read_tag(rs);
    BAR();
    if (lane == 0) { ctl[2] = tg.x; ctl[3] = tg.y; WAIT_LGKM0(); ctl[0] = 1u; }
    wait_preset(rs, lane, tg TUNE_PASS);
    if (lane == 0) ctl[0] = 2u;
    STAMP(2);
    STAMP_DUMP();
    return;
  }
  BAR();
  STAMP(0);
  wslice_dma(p.wq, 8192, 64 * w, (g * 512 + 64 * j) * 2, lbase + L_WA + w * 8192, lane);
  unsigned d[2];
#pragma unroll
  for (int ks = 0; ks < 2; ++ks) d[ks] = *(const GAS unsigned*)(p.x + (c16 >> 2) * 512 + 64 * w + 32 * ks + 8 * grp + 2 * (c16 & 3));
  WAIT_VM(0);
  v4u wq[2][4], wo[4][2];
  wslice_frags(L + L_WA + w * 8192, lane, wq);
  float csq = 0.f;
  int rb = 0;
  unsigned tref = 0;
  int nb = 0, nb0 = 0;
  H2 ea = {{0u, 0u}}, eb = {{0u, 0u}};
  G9 ga = {{0u, 0u, 0u, 0u, 0u, 0u, 0u, 0u, 0u}}, gb = ga;

#pragma unroll LAYER_UNROLL
  for (int layer = 0; layer < 2; ++layer) {
    {
      LAS unsigned char* R = L + L_RED + rb; rb ^= RED_BUF;
      if (layer == 1) {
        if (nb0) nb = near_copy(rs, g, ctl[2], ld8(rs, WS_HINT + HN * 1024 + g * 128));
        if (w == 0) { const unsigned th = lds_clock(ctl + 8); CLK(24, th); wait_clock(th + TP(D_E)); }
        BAR();
        int it = 0;
        const int xo = WS_X2 + nb + (4 * w + (grp >> 1)) * 128 + (c16 >> 2) * 32 + (grp & 1) * 16 + (c16 & 3) * 4;
        H2 dd = ea;
#define LD_E(r_) do { r_.v[0] = ld4(rs, xo); r_.v[1] = ld4(rs, xo + 256); } while (0)
#define OK_E(r_) (max(r_.v[0], r_.v[1]) != SENT)
        POLL2(dd, ea, eb, GAP_E, LD_E, OK_E, it);
        d[0] = dd.v[0]; d[1] = dd.v[1];
        STAMP(10); NPASS(20, it);
      }
      CY(2);
      const v4u xa[2] = {quad_frag(d[0]), quad_frag(d[1])};
      float sxx = dot2(d[1], d[1], dot2(d[0], d[0], 0.f)), sx = dot2(d[1], 0x3F803F80u, dot2(d[0], 0x3F803F80u, 0.f));
      proj_partial(R, w, lane, xa, wq);
      sxx += dpp<XOR1>(sxx); sx += dpp<XOR1>(sx);
      sxx += dpp<XOR2>(sxx); sx += dpp<XOR2>(sx);
      sxx = xrow_sum(sxx); sx = xrow_sum(sx);
      if (lane < 16 && (c16 & 3) == 0) *(LAS f32x2*)(R + RED_G + w * 32 + (c16 >> 2) * 8) = f32x2{sxx, sx};
      if (layer == 0) colsum_partial(R, w, lane, wq);
      if (layer == 1) { tref = clock32_now(); CLK(25, tref); }
      CY(3);
      BAR();
      CY(4);
      if (w < 4) {
        if (layer == 0) csq = colsum_final(R, lane);
        const float y = ln_proj_final(R, w, lane, lane, csq);
        if (layer == 0) {
          ctl_wait(ctl, 1u);
          const unsigned t0 = ctl[2], t1 = ctl[3];
          st8(rs, WS_Q0 + qid * 2048 + w * 512 + lane * 8, v2u{(pk(y, y) & 0xFFFFu) | (t0 << 16), (t0 >> 16) | (t1 << 16)});
        } else {
          publish_pairs(rs, myout + 512 + w * 128, lane, y);
          CY(6);
        }
        STAMP(layer == 0 ? 1 : 11);
      }
    }
    KEEP(ea.v[0]); KEEP(ea.v[1]); KEEP(eb.v[0]); KEEP(eb.v[1]);
    if (layer == 0) {
      PACE(DLY_WO);
      const v2u hn = ld8(rs, WS_HINT + HN * 1024 + g * 128);
      wslice_dma(p.wo, 1024, 512 * g + 64 * w, 128 * j, lbase + L_WB + w * 8192, lane);
      WAIT_VM(0);
      BAR();
      {
        const int q = c16 >> 2, pp = lane & 3;
#pragma unroll
        for (int ks = 0; ks < 4; ++ks)
#pragma unroll
          for (int nt = 0; nt < 2; ++nt) {
            v2u hv[2];
#pragma unroll
            for (int t = 0; t < 2; ++t) {
              const int r = 32 * (ks & 1) + 8 * grp + 4 * t + q;
              const int sw = (((r >> 1) & 1) | (((r >> 3) & 1) << 1)) << 1;
              hv[t] = trread(L + L_WB + ((w & 6) + (ks >> 1)) * 8192 + r * 128 + (((2 * (2 * (w & 1) + nt) + (pp >> 1)) ^ sw) << 4) + (pp & 1) * 8);
            }
            wo[ks][nt] = v4u{hv[0].x, hv[0].y, hv[1].x, hv[1].y};
          }
      }
      STAMP(2);
      ctl_wait(ctl, 2u);
      nb = __builtin_amdgcn_readfirstlane(hn.y) == ctl[2] ? (int)(((__builtin_amdgcn_readfirstlane(hn.x) ^ my_side()) & 1u) * CP) : -1;
      if (nb < 0) nb0 = 1, nb = 0;
      STAMP(3);
    } else {
      if (w == 0) wait_clock(tref + TP(D_B));
      BAR();
    }
    {
      LAS unsigned char* R = L + L_RED + rb; rb ^= RED_BUF;
      const int hh = w >> 1;
      v4u og[2];
      unsigned lg4;
      const int po = po_slot(g * 16 + 2 * w) + nb + layer * 1024 + lane * 16;
      const int ps = ps_slot(g * 16 + 2 * w) + nb + ((lane >> 2) & 1) * 256 + layer * 128 + (lane & 3) * 4;
      STAMP(layer == 0 ? 19 : 18);
      int it = 0;
      if (layer == 0 && B0_POLL2) {
        G9 gg = ga;
#define LD_B(r_) do { const v4u t0_ = ld16(rs, po), t1_ = ld16(rs, po + 2048); r_.v[8] = ld4(rs, ps); \
          r_.v[0] = t0_.x; r_.v[1] = t0_.y; r_.v[2] = t0_.z; r_.v[3] = t0_.w; r_.v[4] = t1_.x; r_.v[5] = t1_.y; r_.v[6] = t1_.z; r_.v[7] = t1_.w; } while (0)
#define OK_B(r_) (max(max(max(max(r_.v[0], r_.v[1]), max(r_.v[2], r_.v[3])), max(max(r_.v[4], r_.v[5]), max(r_.v[6], r_.v[7]))), r_.v[8]) != SENT)
        POLL2(gg, ga, gb, GAP_B, LD_B, OK_B, it);
        og[0] = v4u{gg.v[0], gg.v[1], gg.v[2], gg.v[3]}; og[1] = v4u{gg.v[4], gg.v[5], gg.v[6], gg.v[7]}; lg4 = gg.v[8];
      } else {
        _Pragma("nounroll") for (; it < SPIN_CAP; ++it) {
          FRESH();
          og[0] = ld16(rs, po);
          og[1] = ld16(rs, po + 2048);
          lg4 = ld4(rs, ps);
          if (all_lanes(max(max(mx4(og[0]), mx4(og[1])), lg4) != SENT)) break;
          BACKOFF();
        }
      }
      if (LITE == 2) { CLK(26 + layer, clock32()); }
      STAMP(layer == 0 ? 4 : 12); NPASS(layer == 0 ? 21 : 22, it);
      CY(7);
      LAS unsigned char* S = L + L_WA;
      *(LAS v4u*)(S + (2 * w) * 1056 + lane * 16) = og[0];
      *(LAS v4u*)(S + (2 * w + 1) * 1056 + lane * 16) = og[1];
      if (lane < 8) *(LAS unsigned*)(S + 16 * 1056 + 2 * w * 16 + lane * 4) = lg4;
      CY(8);
      BAR();
      CY(9);
      if (j == 0 && w == 7) { const unsigned th = clock32_now(); if (lane < 2) st8(rs, WS_HINT + lane * CP + (HB0 + layer) * 1024 + g * 128, v2u{th, ctl[2]}); }
      const unsigned lw = *(LAS unsigned*)(S + 16 * 1056 + (4 * (c16 & 3) + grp) * 16 + hh * 4);
      v4u oa[4];
#pragma unroll
      for (int ks = 0; ks < 4; ++ks)
        oa[ks] = *(LAS v4u*)(S + (4 * (c16 & 3) + (c16 >> 2)) * 1056 + (2 * ks + (grp >> 1)) * 128 + hh * 32 + (grp & 1) * 16);
      __builtin_amdgcn_sched_barrier(0);
      f32x4 acc[2];
#pragma unroll
      for (int nt = 0; nt < 2; ++nt)
        acc[nt] = mfma(oa[3], wo[3][nt], mfma(oa[2], wo[2][nt], mfma(oa[1], wo[1][nt], mfma(oa[0], wo[0][nt], f32x4{0.f, 0.f, 0.f, 0.f}))));
      const float lg = __uint_as_float(lw);
      const float e = __builtin_amdgcn_exp2f(lg - xrow_max(lg));
      const float wt1 = e * __builtin_amdgcn_rcpf(xrow_sum(e));
      const float wt[4] = {dpp<0x00>(wt1), dpp<0x55>(wt1), dpp<0xAA>(wt1), dpp<0xFF>(wt1)};
      {
        float u[4];
#pragma unroll
        for (int r = 0; r < 4; ++r) u[r] = swap32_add(acc[0][r] * wt[r], acc[1][r] * wt[r]);
        const int n = 32 * (w & 1) + 16 * (grp >> 1) + c16;
        *(LAS float*)(R + hh * 1024 + (grp & 1) * 256 + n * 4) = swap16_add(u[0], u[1]);
        *(LAS float*)(R + hh * 1024 + (2 + (grp & 1)) * 256 + n * 4) = swap16_add(u[2], u[3]);
      }
      CY(14);
      BAR();
      CY(15);
      if (w < 4) {
        float t[4];
#pragma unroll
        for (int v = 0; v < 4; ++v) t[v] = *(LAS float*)(R + v * 1024 + (2 * (w & 1) + (lane >> 5)) * 256 + (32 * (w >> 1) + (lane & 31)) * 4);
        __builtin_amdgcn_sched_barrier(0);
        publish_pairs(rs, myout + 1024 + layer * 512 + w * 128, lane, (t[0] + t[1]) + (t[2] + t[3]));
        CY(16);
        STAMP(layer == 0 ? 5 : 13);
      }
      if (layer == 0 && B0_POLL2) {
#pragma unroll
        for (int i = 0; i < 9; ++i) { KEEP(ga.v[i]); KEEP(gb.v[i]); }
      }
      if (layer == 0 && w == 7) { PACE(DLY_HQ); fetch_hint(ctl + 8, rs, lane, ctl[2], HC0, g, nb); }
    }
  }
  if (qid == 0 && w == 0 && lane == 0) st8(rs, WS_EPOCH, v2u{ctl[2], ctl[3]});
  STAMP_DUMP();
}

__device__ __forceinline__ void f1_role(const Params& p, LAS unsigned char* L, unsigned lbase, __amdgpu_buffer_rsrc_t rs, int f, int w, int lane TUNE_ARG) {
  volatile LAS unsigned* ctl = (volatile LAS unsigned*)(L + L_CTL);
  const int c16 = lane & 15, grp = lane >> 4, b4 = lane & 3;
  const int myout = h_slot(f);
  if (w == 8) {
    control_announce(p, L, lbase, rs, lane, f, f >> 2, f & 3 TUNE_PASS);
    STAMP(2);
    STAMP_DUMP();
    return;
  }
  preset_chunk(rs, f, w * 64 + lane);
  touch_pages(p, lbase, f >> 2, f & 3, w, lane);
  warm_k(p, lbase, f >> 2, f & 3, w, lane);
  if (f >= 28) {
    if (WARM_O) { PACE(DLY_WOW); warm_o(p, lbase, f >> 2, f & 3, w, lane); WAIT_VM(0); }
    return;
  }
  STAMP(0);
  const int xcol = 32 * (2 * w + (grp >> 1)) + 8 * (c16 >> 2) + 4 * (grp & 1);
  v2u xr = *(const GAS v2u*)(p.x + b4 * 512 + xcol);
  if (WARM_O) { PACE(DLY_WOW); warm_o(p, lbase, f >> 2, f & 3, w, lane); PACE_DIFF(DLY_W1, DLY_WOW); }
  else PACE(DLY_W1);
  const v2u hn = ld8(rs, WS_HINT + HN * 1024 + (f >> 2) * 128);
  wslice_dma(p.w1, 3584, 64 * w, 128 * f, lbase + L_WA + w * 8192, lane);
  WAIT_VM(0);
  BAR();
  v4u w1f[16];
  float cs = 0.f;
  if (w < 4) {
    const int q = c16 >> 2, pp = lane & 3;
#pragma unroll
    for (int ks = 0; ks < 16; ++ks) {
      v2u hv[2];
#pragma unroll
      for (int t = 0; t < 2; ++t) {
        const int r = 32 * (ks & 1) + 8 * grp + 4 * t + q;
        const int sw = (((r >> 1) & 1) | (((r >> 3) & 1) << 1)) << 1;
        hv[t] = trread(L + L_WA + (ks >> 1) * 8192 + r * 128 + (((2 * w + (pp >> 1)) ^ sw) << 4) + (pp & 1) * 8);
      }
      w1f[ks] = v4u{hv[0].x, hv[0].y, hv[1].x, hv[1].y};
    }
    const v4u ones = {0x3F803F80u, 0x3F803F80u, 0x3F803F80u, 0x3F803F80u};
    f32x4 c = {0.f, 0.f, 0.f, 0.f};
#pragma unroll
    for (int ks = 0; ks < 16; ++ks) c = mfma(ones, w1f[ks], c);
    cs = c[0];
  } else {
#pragma unroll
    for (int ks = 0; ks < 16; ++ks) w1f[ks] = v4u{0u, 0u, 0u, 0u};
  }
  v4u sel;
  {
    const int r = c16 & 7;
    const unsigned v = (r & 1) ? 0x3F800000u : 0x00003F80u;
    sel = v4u{(r >> 1) == 0 ? v : 0u, (r >> 1) == 1 ? v : 0u, (r >> 1) == 2 ? v : 0u, (r >> 1) == 3 ? v : 0u};
  }
  STAMP(2);
  ctl_wait(ctl, 2u);
  STAMP(3);
  const int nb = near_copy(rs, f >> 2, ctl[2], hn);
  unsigned tseen = 0;
#pragma unroll LAYER_UNROLL
  for (int layer = 0; layer < 2; ++layer) {
    LAS unsigned char* R = L + L_RED + (1 - layer) * RED_BUF;
    if (layer == 1) {
      wait_clock(tseen + TP(D_X));
      int it = 0;
      _Pragma("nounroll") for (; it < SPIN_CAP; ++it) {
        FRESH();
        xr = ld8(rs, WS_X2 + nb + (xcol >> 4) * 128 + b4 * 32 + (xcol & 15) * 2);
        if (all_lanes(max(xr.x, xr.y) != SENT)) break;
        __builtin_amdgcn_s_sleep(2);
      }
      STAMP(10); NPASS(20, it);
    }
    if (w == 7) {
      const unsigned th = fetch_hint(ctl + 8 + layer, rs, lane, ctl[2], HB0 + layer, -1, nb);
      STAMP(layer == 0 ? 4 : 5); CLK(24 + layer, th);
      wait_clock(th + (layer == 0 ? TP(D_C0) : TP(D_C1)));
    }
    BAR();
    {
      const f32x4 xc = {bflo(xr.x), bfhi(xr.x), bflo(xr.y), bfhi(xr.y)};
      v4u pc[2][2];
      STAMP(layer == 0 ? 19 : 18);
      int it = 0;
      _Pragma("nounroll") for (; it < SPIN_CAP; ++it) {
        FRESH();
#pragma unroll
        for (int u = 0; u < 2; ++u)
#pragma unroll
          for (int hf = 0; hf < 2; ++hf)
            pc[u][hf] = ld16(rs, qo_slot((grp + 4 * hf) * 8 + w) + nb + 1024 + layer * 512 + u * 256 + b4 * 64 + (c16 >> 2) * 16);
        if (all_lanes(max(max(mx4(pc[0][0]), mx4(pc[0][1])), max(mx4(pc[1][0]), mx4(pc[1][1]))) != SENT)) break;
        BACKOFF();
      }
      STAMP(layer == 0 ? 6 : 14); NPASS(layer == 0 ? 21 : 22, it);
      f32x4 s0 = mfma(sel, pc[0][0], xc), s1 = mfma(sel, pc[1][0], xc);
      s0 = mfma(sel, pc[0][1], s0);
      s1 = mfma(sel, pc[1][1], s1);
      const f32x4 x1 = (grp < 2) ? s0 : s1;
      const unsigned e0 = pk(x1[0], x1[1]), e1 = pk(x1[2], x1[3]);
      *(LAS v2u*)(L + L_ACT + b4 * ACT_ROW + xcol * 2) = v2u{e0, e1};
      float sxx = dot2(e1, e1, dot2(e0, e0, 0.f)), sx = dot2(e1, 0x3F803F80u, dot2(e0, 0x3F803F80u, 0.f));
      sxx += dpp<ROR4>(sxx); sx += dpp<ROR4>(sx);
      sxx += dpp<ROR8>(sxx); sx += dpp<ROR8>(sx);
      sxx = xrow_sum(sxx); sx = xrow_sum(sx);
      if (lane < 4) *(LAS f32x2*)(R + RED_G + w * 32 + lane * 8) = f32x2{sxx, sx};
      tseen = clock32_now();
      CLK(26 + layer, tseen);
    }
    BAR();
    if (layer == 0 && f == 0 && w == 7 && lane < 16) st8(rs, WS_HINT + (lane >> 3) * CP + HC0 * 1024 + (lane & 7) * 128, v2u{tseen, ctl[2]});
    if (w < 4) {
      f32x2 gs = *(LAS f32x2*)(R + RED_G + (lane & 7) * 32 + grp * 8);
      v4u xa[16];
#pragma unroll
      for (int ks = 0; ks < 12; ++ks) xa[ks] = *(LAS v4u*)(L + L_ACT + (c16 >> 2) * ACT_ROW + (32 * ks + 8 * grp) * 2);
      __builtin_amdgcn_sched_barrier(0);
      f32x4 a0 = mfma(xa[0], w1f[0], f32x4{0.f, 0.f, 0.f, 0.f}), a1 = mfma(xa[1], w1f[1], f32x4{0.f, 0.f, 0.f, 0.f});
      __builtin_amdgcn_sched_barrier(0);
#pragma unroll
      for (int ks = 12; ks < 16; ++ks) xa[ks] = *(LAS v4u*)(L + L_ACT + (c16 >> 2) * ACT_ROW + (32 * ks + 8 * grp) * 2);
      __builtin_amdgcn_sched_barrier(0);
#pragma unroll
      for (int ks = 2; ks < 16; ks += 2) { a0 = mfma(xa[ks], w1f[ks], a0); a1 = mfma(xa[ks + 1], w1f[ks + 1], a1); }
      float h = ln_apply(gs, a0[0] + a1[0], cs);
      h = bflo(pk(h, h));
      publish_pairs(rs, myout + layer * 512 + w * 128, lane, h * __builtin_amdgcn_rcpf(1.0f + __expf(-h)));
      STAMP(layer == 0 ? 7 : 15);
    }
  }
  STAMP_DUMP();
}

struct H7 { unsigned v[7]; };
__device__ __forceinline__ void f2_role(const Params& p, LAS unsigned char* L, unsigned lbase, __amdgpu_buffer_rsrc_t rs, int p2, int w, int lane TUNE_ARG) {
  volatile LAS unsigned* ctl = (volatile LAS unsigned*)(L + L_CTL);
  const int c16 = lane & 15, grp = lane >> 4;
  const int myout = WS_X2 + p2 * 128;
  if (w == 8) {
    control_announce(p, L, lbase, rs, lane, 32 + p2, p2 >> 2, 4 + (p2 & 3) TUNE_PASS);
    STAMP(2);
    STAMP_DUMP();
    return;
  }
  preset_chunk(rs, 32 + p2, w * 64 + lane);
  touch_pages(p, lbase, p2 >> 2, 4 + (p2 & 3), w, lane);
  warm_k(p, lbase, p2 >> 2, 4 + (p2 & 3), w, lane);
  STAMP(0);
  const int rb_ = (lane >> 1) & 3, rh = lane & 1;
  v4u xin = {0u, 0u, 0u, 0u};
  if (w == 1) xin = *(const GAS v4u*)(p.x + rb_ * 512 + 16 * p2 + 8 * rh);
  if (WARM_O) { PACE(DLY_WOW); warm_o(p, lbase, p2 >> 2, 4 + (p2 & 3), w, lane); PACE_DIFF(DLY_W2, DLY_WOW); }
  else PACE(DLY_W2);
  if (W2_WARM) {
#pragma unroll
    for (int i = 0; i < 7; ++i) {
      unsigned keep;
      const unsigned off = (unsigned)((448 * (p2 & 3) + 56 * w + 8 * i + (lane >> 3)) * 1024 + 128 * (p2 >> 2) + (lane & 7) * 16);
      asm volatile("s_mov_b32 %0, m0\n\ts_mov_b32 m0, %3\n\ts_nop 0\n\tglobal_load_lds_dwordx4 %1, %2\n\ts_mov_b32 m0, %0"
                   : "=&s"(keep) : "v"(off), "s"(p.w2), "s"(lbase + L_SCR) : "memory");
    }
    WAIT_VM(0);
    PACE(DLY_W2B);
  }
  const v2u hn = ld8(rs, WS_HINT + HN * 1024 + (p2 >> 2) * 128);
#pragma unroll
  for (int i = 0; i < 7; ++i) {
    const int rp = lane >> 1, r = rp ^ (((rp >> 3) & 1) << 2);
    dma16(p.w2, (unsigned)((224 * w + 32 * i + r) * 1024 + 32 * p2 + (lane & 1) * 16), lbase + L_WA + w * 8192 + i * 1024);
  }
  WAIT_VM(0);
  v4u w2f[7];
  {
    const int q = c16 >> 2, pp = lane & 3;
#pragma unroll
    for (int i = 0; i < 7; ++i) {
      v2u h[2];
#pragma unroll
      for (int t = 0; t < 2; ++t) {
        const int r = 8 * grp + 4 * t + q, rp = r ^ (((r >> 3) & 1) << 2);
        h[t] = trread(L + L_WA + w * 8192 + i * 1024 + rp * 32 + pp * 8);
      }
      w2f[i] = v4u{h[0].x, h[0].y, h[1].x, h[1].y};
    }
  }
  STAMP(2);
  ctl_wait(ctl, 2u);
  STAMP(3);
  const int nb = near_copy(rs, p2 >> 2, ctl[2], hn);
  int hoff[7];
#pragma unroll
  for (int i = 0; i < 7; ++i) {
    const int k0 = 224 * w + 32 * i;
    hoff[i] = h_slot(k0 >> 6) + nb + (((k0 & 63) >> 4) + (grp >> 1)) * 128 + (c16 >> 2) * 32 + (grp & 1) * 16 + (c16 & 3) * 4;
  }
  H7 ha, hb;
#pragma unroll
  for (int i = 0; i < 7; ++i) { ha.v[i] = 0u; hb.v[i] = 0u; }
  float xs[8];
#pragma unroll
  for (int i = 0; i < 4; ++i) { xs[2 * i] = bflo(xin[i]); xs[2 * i + 1] = bfhi(xin[i]); }
#pragma unroll LAYER_UNROLL
  for (int layer = 0; layer < 2; ++layer) {
    LAS unsigned char* R = L + L_RED + layer * RED_BUF;
    LAS unsigned char* X1 = L + L_ACT + layer * 256;
    unsigned tw = 0;
    if (w == 1) {
      wait_clock(fetch_hint(ctl + 8 + layer, rs, lane, ctl[2], HB0 + layer, -1, nb) + (layer == 0 ? TP(D_C0) : TP(D_C1)));
      v4u pc;
      int it = 0;
      _Pragma("nounroll") for (; it < SPIN_CAP; ++it) {
        FRESH();
        pc = ld16(rs, qo_slot((lane >> 3) * 8 + (p2 >> 2)) + nb + 1024 + layer * 512 + ((p2 & 3) >> 1) * 256 + rb_ * 64 + (p2 & 1) * 32 + rh * 16);
        if (all_lanes(mx4(pc) != SENT)) break;
        __builtin_amdgcn_s_sleep(2);
      }
      tw = clock32();
      CLK(24 + layer, tw);
      STAMP(layer == 0 ? 6 : 14); NPASS(layer == 0 ? 20 : 23, it);
      float v[8];
#pragma unroll
      for (int i = 0; i < 4; ++i) { v[2 * i] = bflo(pc[i]); v[2 * i + 1] = bfhi(pc[i]); }
#pragma unroll
      for (int i = 0; i < 8; ++i) { float t = v[i] + dpp<ROR8>(v[i]); t = xrow_sum(t); const unsigned r2 = pk(xs[i] + t, 0.f); xs[i] = bflo(r2); }
      if (lane < 8) {
        *(LAS f32x4*)(X1 + rb_ * 64 + rh * 32) = f32x4{xs[0], xs[1], xs[2], xs[3]};
        *(LAS f32x4*)(X1 + rb_ * 64 + rh * 32 + 16) = f32x4{xs[4], xs[5], xs[6], xs[7]};
      }
    }
    if (w == 1) wait_clock(tw + (layer == 0 ? TP(D_D0) : TP(D_D1)));
    BAR();
    STAMP(layer == 0 ? 19 : 18);
    int it = 0;
    H7 d = ha;
#define LD_D(x) do { _Pragma("unroll") for (int i = 0; i < 7; ++i) x.v[i] = ld4(rs, hoff[i] + layer * 512); } while (0)
#define OK_D(x) (max(max(max(x.v[0], x.v[1]), max(x.v[2], x.v[3])), max(max(x.v[4], x.v[5]), x.v[6])) != SENT)
    POLL2(d, ha, hb, GAP_D, LD_D, OK_D, it);
    if (LITE == 2) { CLK(26 + layer, clock32()); }
    STAMP(layer == 0 ? 8 : 16); NPASS(layer == 0 ? 21 : 22, it);
    f32x4 a0 = {0.f, 0.f, 0.f, 0.f}, a1 = a0;
v4u hf[7];
#pragma unroll
    for (int i = 0; i < 7; ++i) hf[i] = quad_frag(d.v[i]);
    __builtin_amdgcn_sched_group_barrier(0x002, 8, 0);
#pragma unroll
    for (int i = 0; i < 6; ++i) { __builtin_amdgcn_sched_group_barrier(0x008, 1, 0); __builtin_amdgcn_sched_group_barrier(0x002, 4, 0); }
    __builtin_amdgcn_sched_group_barrier(0x008, 1, 0);
#pragma unroll
    for (int i = 0; i < 7; ++i) { if (i & 1) a1 = mfma(hf[i], w2f[i], a1); else a0 = mfma(hf[i], w2f[i], a0); }
    *(LAS float*)(R + w * 256 + grp * 64 + c16 * 4) = a0[0] + a1[0];
    if (layer == 0 && p2 == 0 && w == 7) { const unsigned th = clock32_now(); if (lane < 16) st8(rs, WS_HINT + (lane >> 3) * CP + HD0 * 1024 + (lane & 7) * 128, v2u{th, ctl[2]}); }
    BAR();
    if (w == 0) {
      float t[8];
#pragma unroll
      for (int v = 0; v < 8; ++v) t[v] = *(LAS float*)(R + v * 256 + lane * 4);
      const float xr = *(LAS float*)(X1 + lane * 4);
      __builtin_amdgcn_sched_barrier(0);
      const float y = xr + (((t[0] + t[1]) + (t[2] + t[3])) + ((t[4] + t[5]) + (t[6] + t[7])));
      const unsigned o = pk(y, dpp<XOR1>(y));
      if ((lane & 1) == 0) {
        if (layer == 0) { st4(rs, myout + lane * 2, canon(o)); st4(rs, myout + CP + lane * 2, canon(o)); *(LAS unsigned*)(L + L_ACT + 1024 + lane * 2) = o; }
        else if (OUT_SC1) __hip_atomic_store(p.out + (lane >> 4) * 256 + 8 * p2 + ((lane & 15) >> 1), o, __ATOMIC_RELAXED, __HIP_MEMORY_SCOPE_AGENT);
        else p.out[(lane >> 4) * 256 + 8 * p2 + ((lane & 15) >> 1)] = o;
      }
      STAMP(layer == 0 ? 9 : 17);
    }
#pragma unroll
    for (int i = 0; i < 7; ++i) { KEEP(ha.v[i]); KEEP(hb.v[i]); }
    if (layer == 0) {
      BAR();
      if (w == 1) {
        const v4u x2 = *(LAS v4u*)(L + L_ACT + 1024 + rb_ * 32 + rh * 16);
#pragma unroll
        for (int i = 0; i < 4; ++i) { xs[2 * i] = bflo(x2[i]); xs[2 * i + 1] = bfhi(x2[i]); }
      }
    }
  }
  STAMP_DUMP();
}

__device__ __forceinline__ void attn_role(const Params& p, LAS unsigned char* L, unsigned lbase, __amdgpu_buffer_rsrc_t rs, int g, int idx, int w, int lane TUNE_ARG) {
  volatile LAS unsigned* ctl = (volatile LAS unsigned*)(L + L_CTL);
  const int c16 = lane & 15, grp = lane >> 4;
  const int b = idx >> 2, s = idx & 3;
  const int aid = g * 16 + idx;
  const int myout = po_slot(aid);

  if (w == 8) {
    const unsigned t_entry = clock32();
    const v2u tg = read_tag(rs);
    BAR();
    if (lane == 0) { ctl[2] = tg.x; ctl[3] = tg.y; }
    BAR();
    const unsigned thx = tg.x << 16, thi = (tg.x >> 16) | (tg.y << 16);
    v4u q[4];
    wait_clock(t_entry + TP(T_Q0));
    _Pragma("nounroll") for (int it = 0; it < SPIN_CAP; ++it) {
      FRESH();
#pragma unroll
      for (int i = 0; i < 4; ++i) q[i] = ld16(rs, WS_Q0 + (g * 8 + 2 * i + (lane >> 5)) * 2048 + b * 512 + (lane & 31) * 16);
      unsigned bad = 0u;
#pragma unroll
      for (int i = 0; i < 4; ++i) bad |= ((q[i].x ^ thx) | (q[i].z ^ thx)) >> 16 | (q[i].y ^ thi) | (q[i].w ^ thi);
      if (all_lanes(bad == 0u)) break;
    }
#pragma unroll
    for (int i = 0; i < 4; ++i) *(LAS unsigned*)(L + L_QL + i * 320 + lane * 4) = (q[i].x & 0xFFFFu) | (q[i].z << 16);
    BAR();
    STAMP(3);
    STAMP_DUMP();
    return;
  }
  BAR();
  STAMP(0);
  const size_t kvoff = ((size_t)(b * 8 + g) * 4096 + 3072 + 256 * s + 32 * w) * 256;
  const unsigned char* kb = p.kc + kvoff;
  const unsigned char* vb = p.vc + kvoff;
  v4u kwin0, kwin1, kwin2, kwin3, kwin4, kwin5, kwin6, kwin7;
  PACE(DLY_K);
  if (KV_DMA) {
#pragma unroll
    for (int i = 0; i < 8; ++i) {
      const int row = 4 * i + grp;
      dma16(vb, (unsigned)(row * 256 + ((c16 ^ ((row & 7) << 1)) * 16)), lbase + L_V + w * 8192 + i * 1024);
    }
#pragma unroll
    for (int i = 0; i < 8; ++i) {
      const int row = 4 * i + grp;
      WAIT_VM(7);
      dma16(kb, (unsigned)(row * 256 + ((c16 ^ (row & 15)) * 16)), lbase + L_K + w * 8192 + i * 1024);
    }
  } else {
    v4u win[8];
#pragma unroll
    for (int i = 0; i < 8; ++i) win[i] = __builtin_nontemporal_load((const GAS v4u*)(vb + i * 1024 + lane * 16));
    __builtin_amdgcn_sched_barrier(0);
#pragma unroll
    for (int i = 0; i < 8; ++i) {
      const int row = 4 * i + grp;
      *(LAS v4u*)(L + L_V + w * 8192 + row * 256 + ((c16 ^ ((row & 7) << 1)) * 16)) = win[i];
      win[i] = __builtin_nontemporal_load((const GAS v4u*)(kb + i * 1024 + lane * 16));
      __builtin_amdgcn_sched_barrier(0);
    }
    kwin0 = win[0]; kwin1 = win[1]; kwin2 = win[2]; kwin3 = win[3]; kwin4 = win[4]; kwin5 = win[5]; kwin6 = win[6]; kwin7 = win[7];
  }
  BAR();
  v4u kf[2][4], vf[8], qf[4];
  {
    const int q4 = c16 >> 2, pp = lane & 3;
#pragma unroll
    for (int v = 0; v < 8; ++v) {
      const int r0 = 4 * grp + q4;
      const int sl = ((2 * w + (pp >> 1)) ^ ((r0 & 7) << 1)) * 16 + (pp & 1) * 8;
      LAS unsigned char* a = L + L_V + v * 8192 + r0 * 256 + sl;
      vf[v] = trfrag(a, a + 4096);
    }
  }
  if (KV_DMA) {
    WAIT_VM(0);
  } else {
    const v4u kw[8] = {kwin0, kwin1, kwin2, kwin3, kwin4, kwin5, kwin6, kwin7};
#pragma unroll
    for (int i = 0; i < 8; ++i) {
      const int row = 4 * i + grp;
      *(LAS v4u*)(L + L_K + w * 8192 + row * 256 + ((c16 ^ (row & 15)) * 16)) = kw[i];
    }
    WAIT_LGKM0();
  }
#pragma unroll
  for (int t = 0; t < 2; ++t)
#pragma unroll
    for (int st = 0; st < 4; ++st)
      kf[t][st] = *(LAS v4u*)(L + L_K + w * 8192 + (16 * t + c16) * 256 + (((4 * st + grp) ^ c16) * 16));
  STAMP(1);
  v4u fl = {0u, 0u, 0u, 0u};
  if (w == 7) fl = ld16(rs, WS_FL + (lane & 31) * 16);
  BAR();
  const int h = c16 >> 2;
#pragma unroll
  for (int st = 0; st < 4; ++st) qf[st] = *(LAS v4u*)(L + L_QL + h * 320 + st * 64 + grp * 16);
  __builtin_amdgcn_sched_barrier(0);
  constexpr float CS = 0.08838834764831845f * 1.4426950408889634f;
  H4 qa = {{0u, 0u, 0u, 0u}}, qb = qa;

#pragma unroll LAYER_UNROLL
  for (int layer = 0; layer < 2; ++layer) {
    if (layer == 1) {
      if (w == 0) {
        PACE(DLY_HA);
        const int nb = near_copy(rs, g, ctl[2], ld8(rs, WS_HINT + HN * 1024 + g * 128));
        { const unsigned th = fetch_hint(ctl + 8, rs, lane, ctl[2], HD0, g, nb); CLK(25, th); wait_clock(th + TP(D_A)); }
        H4 q = qa;
        int it = 0;
        const int qo = qo_slot(g * 8 + (lane >> 3)) + nb + 512 + b * 128 + (lane & 7) * 16;
#define LD_A(r_) do { const v4u t_ = ld16(rs, qo); r_.v[0] = t_.x; r_.v[1] = t_.y; r_.v[2] = t_.z; r_.v[3] = t_.w; } while (0)
#define OK_A(r_) (max(max(r_.v[0], r_.v[1]), max(r_.v[2], r_.v[3])) != SENT)
        POLL2(q, qa, qb, GAP_A, LD_A, OK_A, it);
        NPASS(28, it);
        *(LAS v4u*)(L + L_QL + (lane >> 4) * 320 + (lane & 15) * 16) = v4u{q.v[0], q.v[1], q.v[2], q.v[3]};
      }
      BAR();
#pragma unroll
      for (int st = 0; st < 4; ++st) qf[st] = *(LAS v4u*)(L + L_QL + h * 320 + st * 64 + grp * 16);
      __builtin_amdgcn_sched_barrier(0);
    }
    STAMP(layer == 0 ? 2 : 5);
    f32x4 sc[2];
#pragma unroll
    for (int t = 0; t < 2; ++t) {
      f32x4 a = {0.f, 0.f, 0.f, 0.f};
#pragma unroll
      for (int st = 0; st < 4; ++st) a = mfma(kf[t][st], qf[st], a);
      sc[t] = a;
    }
    float m = fmx(fmx(fmx(sc[0][0], sc[0][1]), fmx(sc[0][2], sc[0][3])), fmx(fmx(sc[1][0], sc[1][1]), fmx(sc[1][2], sc[1][3])));
    FSTAMP(20);
    m = xrow_max(m) * CS;
    const bool q1 = lane & 1, q2 = lane & 2;
    const float s0 = q2 ? (q1 ? sc[1][2] : sc[1][0]) : (q1 ? sc[0][2] : sc[0][0]);
    const float s1 = q2 ? (q1 ? sc[1][3] : sc[1][1]) : (q1 ? sc[0][3] : sc[0][1]);
    const float p0 = __builtin_amdgcn_exp2f(fmaf(s0, CS, -m)), p1 = __builtin_amdgcn_exp2f(fmaf(s1, CS, -m));
    *(LAS unsigned*)(L + L_PL + w * 256 + lane * 4) = pk(p0, p1);
    float l = p0 + p1;
    l += dpp<XOR1>(l);
    l += dpp<XOR2>(l);
    l = xrow_sum(l);
    if (lane < 16 && (c16 & 3) == 0) *(LAS f32x2*)(L + L_ST + h * 64 + w * 8) = f32x2{m, l};
    FSTAMP(21);
    BAR();
    FSTAMP(22);
    if (layer == 0 && w == 7) {
      const unsigned t0 = ctl[2], t1 = ctl[3];
      _Pragma("nounroll") for (int it = 0; it < SPIN_CAP; ++it) {
        if (all_lanes(fl.x == t0 && fl.y == t1 && fl.z == t0 && fl.w == t1)) break;
        FRESH();
        fl = ld16(rs, WS_FL + (lane & 31) * 16);
      }
      if (lane == 0) ctl[1] = 1u;
    }
    const f32x2 ml = *(LAS f32x2*)(L + L_ST + h * 64 + ((lane & 3) + 4 * (grp & 1)) * 8);
    v4u pv[8];
#pragma unroll
    for (int v = 0; v < 8; ++v) pv[v] = *(LAS v4u*)(L + L_PL + (v * 16 + grp * 4 + h) * 16);
    __builtin_amdgcn_sched_barrier(0);
    f32x4 o[8];
#pragma unroll
    for (int v = 0; v < 8; ++v) o[v] = mfma(vf[v], pv[v], f32x4{0.f, 0.f, 0.f, 0.f});
    float M = fmx(ml.x, dpp<XOR1>(ml.x));
    M = fmx(M, dpp<XOR2>(M));
    { auto t = __builtin_amdgcn_permlane16_swap(__float_as_uint(M), __float_as_uint(M), false, false); M = fmx(__uint_as_float(t[0]), __uint_as_float(t[1])); }
    FSTAMP(23);
    const float f1 = __builtin_amdgcn_exp2f(ml.x - M);
    float Ls = f1 * ml.y;
    Ls += dpp<XOR1>(Ls);
    Ls += dpp<XOR2>(Ls);
    { auto t = __builtin_amdgcn_permlane16_swap(__float_as_uint(Ls), __float_as_uint(Ls), false, false); Ls = __uint_as_float(t[0]) + __uint_as_float(t[1]); }
    const auto ft = __builtin_amdgcn_permlane16_swap(__float_as_uint(f1), __float_as_uint(f1), false, false);
    const float fa = __uint_as_float(ft[0]), fb = __uint_as_float(ft[1]);
    const float f[8] = {dpp<0x00>(fa), dpp<0x55>(fa), dpp<0xAA>(fa), dpp<0xFF>(fa), dpp<0x00>(fb), dpp<0x55>(fb), dpp<0xAA>(fb), dpp<0xFF>(fb)};
    f32x4 acc = f[0] * o[0];
#pragma unroll
    for (int v = 1; v < 8; ++v) acc += f[v] * o[v];
    const float inv = __builtin_amdgcn_rcpf(Ls);
    FSTAMP(24);
    if (layer == 0) ctl_wait(ctl + 1, 1u);
    if ((c16 & 3) == 0) {
      const v2u ob = {canon(pk(acc[0] * inv, acc[1] * inv)), canon(pk(acc[2] * inv, acc[3] * inv))};
      st8(rs, myout + layer * 1024 + w * 128 + h * 32 + grp * 8, ob);
      st8(rs, myout + CP + layer * 1024 + w * 128 + h * 32 + grp * 8, ob);
    }
    if (w == 0 && lane < 16 && (c16 & 3) == 0) {
      const unsigned lw = canon(__float_as_uint(M + __builtin_amdgcn_logf(Ls)));
      st4(rs, ps_slot(aid) + layer * 128 + h * 4, lw);
      st4(rs, ps_slot(aid) + CP + layer * 128 + h * 4, lw);
    }
    if (LITE == 2) { CLK(layer == 0 ? 24 : 26, clock32()); }
    STAMP(layer == 0 ? 4 : 6);
  }
#pragma unroll
  for (int i = 0; i < 4; ++i) { KEEP(qa.v[i]); KEEP(qb.v[i]); }
  STAMP_DUMP();
}

#define ORIENT 0
#define JROT 0
#define CODE_KB 20
__global__ void __launch_bounds__(NTHR) fused_layers_kernel(Params p) {
  extern __shared__ __attribute__((aligned(16))) unsigned char smem[];
  LAS unsigned char* L = (LAS unsigned char*)smem;
  const unsigned lbase = (unsigned)(uintptr_t)L;
  const int tid = threadIdx.x, lane = tid & 63, w = __builtin_amdgcn_readfirstlane(tid >> 6);
  if (CODE_KB > 0 && w < 8 && blockIdx.x < 192) {
    const u64 pc = __builtin_amdgcn_s_getpc() & ~255ull;
    unsigned keep;
#pragma unroll
    for (int i = 0; i < (CODE_KB + 7) / 8; ++i) {
      const int kb = min(w + 8 * i, CODE_KB - 1);
      asm volatile("s_mov_b32 %0, m0\n\ts_mov_b32 m0, %3\n\ts_nop 0\n\tglobal_load_lds_dwordx4 %1, %2\n\ts_mov_b32 m0, %0"
                   : "=&s"(keep) : "v"(kb * 1024 + lane * 16), "s"(pc), "s"(lbase + L_SCR) : "memory");
    }
  }
  const int g = blockIdx.x & 7, idx = blockIdx.x >> 3;
  const __amdgpu_buffer_rsrc_t rs = __builtin_amdgcn_make_buffer_rsrc((void*)p.ws, 0, WS_END, 0x00020000);
  if (tid >= 512 && tid < 528) *(volatile LAS unsigned*)(L + L_CTL + (tid - 512) * 4) = 0u;
  if (idx < 16) attn_role(p, L, lbase, rs, g, idx, w, lane TUNE_PASS);
  else if (idx < 24) qo_role(p, L, lbase, rs, g, (idx - 16 + JROT) & 7, w, lane TUNE_PASS);
  else if (idx < 28) f1_role(p, L, lbase, rs, g * 4 + (idx - 24), w, lane TUNE_PASS);
  else f2_role(p, L, lbase, rs, g * 4 + (idx - 28), w, lane TUNE_PASS);
}

}

extern "C" void kernel_launch(void* const* d_in, const int* in_sizes, int n_in, void* d_out, int out_size, void* d_ws, size_t ws_size,
                              hipStream_t stream) {
  (void)in_sizes; (void)n_in; (void)out_size; (void)ws_size;
  Params p;
  p.x = (const unsigned short*)d_in[0];
  p.kc = (const unsigned char*)d_in[1];
  p.vc = (const unsigned char*)d_in[2];
  p.wq = (const unsigned char*)d_in[3];
  p.wo = (const unsigned char*)d_in[4];
  p.w1 = (const unsigned char*)d_in[6];
  p.w2 = (const unsigned char*)d_in[7];
  p.out = (unsigned*)d_out;
  p.ws = (unsigned char*)d_ws;
  (void)hipFuncSetAttribute((const void*)fused_layers_kernel, hipFuncAttributeMaxDynamicSharedMemorySize, LDS_BYTES);
  hipLaunchKernelGGL(fused_layers_kernel, dim3(NWG), dim3(NTHR), LDS_BYTES, stream, p);
}
```
